```python
import torch

def _softmax(x, axis=-1):
    return torch.softmax(x, dim=axis)
def _gelu(x):
    return torch.nn.functional.gelu(x, approximate="tanh")
def _silu(x):
    return torch.nn.functional.silu(x)
def _sigmoid(x):
    return torch.sigmoid(x)
def _layernorm(x, axis=-1, eps=1e-5):
    m = x.mean(dim=axis, keepdim=True)
    v = x.var(dim=axis, keepdim=True, unbiased=False)
    return (x - m) / torch.sqrt(v + eps)
def _scatter_add(tgt, idx, src, axis=0):
    return tgt.clone().index_add_(axis, idx, src)
def _segment_sum(src, seg, n):
    out = torch.zeros((n,) + src.shape[1:], dtype=src.dtype)
    return out.index_add_(0, seg, src)
def _pad(x, pw):
    flat = tuple(v for pair in reversed(pw) for v in pair)
    return torch.nn.functional.pad(x, flat)

kernel_name = 'recurrent_scan__linear_attn_block__glu_gated_geglu__prompt'

def setup_inputs():

    _w = (lambda a: (lambda t: t.to(torch.bfloat16) if t.is_floating_point() else t)(torch.as_tensor(a)))
    def _A_setup():
        B, S, D = 1, 2048, 640
        return {
            "x": _w((lambda *s: torch.randn(*s, dtype=torch.bfloat16) * 0.05)(B, S, D)),
            "wqkv": _w((lambda *s: torch.randn(*s, dtype=torch.bfloat16) * 0.8)(D, 3 * D)),
            "wo": _w((lambda *s: torch.randn(*s, dtype=torch.bfloat16) * 0.8)(D, D)),
        }
    _A_in = _A_setup()

    def _B_setup():
        B, S, Dm, Df = 1, 2048, 640, 1536
        return {
            "x": _w((lambda *s: torch.randn(*s, dtype=torch.bfloat16) * 0.05)(B, S, Dm)),
            "w_gate": _w((lambda *s: torch.randn(*s, dtype=torch.bfloat16) * 0.2)(Dm, Df) * (Dm ** -0.5)),
            "w_up": _w((lambda *s: torch.randn(*s, dtype=torch.bfloat16) * 0.2)(Dm, Df) * (Dm ** -0.5)),
            "w_down": _w((lambda *s: torch.randn(*s, dtype=torch.bfloat16) * 0.2)(Df, Dm) * (Df ** -0.5)),
        }
    _B_in = _B_setup()

    return {
        "A_x": _A_in["x"],
        "A_wqkv": _A_in["wqkv"],
        "A_wo": _A_in["wo"],
        "B_x": _B_in["x"],
        "B_w_gate": _B_in["w_gate"],
        "B_w_up": _B_in["w_up"],
        "B_w_down": _B_in["w_down"],
    }

def reference(A_x, A_wqkv, A_wo, B_x, B_w_gate, B_w_up, B_w_down):
    def _A_ref(x, wqkv, wo):
        B, S, D = x.shape
        H = 32; Dh = D // H
        qkv = (lambda x, *d: x.permute(*d))((x @ wqkv).reshape(B, S, 3, H, Dh), 2, 0, 3, 1, 4)
        q, k, v = qkv[0], qkv[1], qkv[2]
        phi = lambda t: torch.where(t > 0, t + 1, torch.exp(t)) + 1e-6
        pq, pk = phi(q), phi(k)
        kv = torch.einsum("bhsd,bhse->bhde", pk, v)
        z = torch.einsum("bhsd->bhd", pk)
        num = torch.einsum("bhqd,bhde->bhqe", pq, kv)
        den = torch.einsum("bhqd,bhd->bhq", pq, z)[..., None] + 1e-6
        o = num / den
        return (lambda x, *d: x.permute(*d))(o, 0, 2, 1, 3).reshape(B, S, D) @ wo

    def _B_ref(x, w_gate, w_up, w_down):
        g = torch.einsum("bsd,df->bsf", x, w_gate)
        u = torch.einsum("bsd,df->bsf", x, w_up)
        h = _gelu(g) * u
        return torch.einsum("bsf,fd->bsd", h, w_down)

    x = A_x
    xn = _layernorm(x, axis=-1)
    s = _A_ref(xn, A_wqkv, A_wo)
    g = _silu(_B_ref(xn, B_w_gate, B_w_up, B_w_down))
    return x + s * g
```

```cpp
#include <hip/hip_runtime.h>
#include <stdint.h>
#include <utility>

typedef __bf16 bf16;
typedef __bf16 v8bf __attribute__((ext_vector_type(8)));
typedef __bf16 v4bf __attribute__((ext_vector_type(4)));
typedef __bf16 v2bf __attribute__((ext_vector_type(2)));
typedef float f32x4 __attribute__((ext_vector_type(4)));
typedef float f32x2 __attribute__((ext_vector_type(2)));
typedef unsigned u32x4 __attribute__((ext_vector_type(4)));
typedef unsigned u32x2 __attribute__((ext_vector_type(2)));
typedef unsigned long long u64;
#define LAS __attribute__((address_space(3)))
#define GAS __attribute__((address_space(1)))
#define DEVI __device__ __forceinline__
#define SB __builtin_amdgcn_sched_barrier(0)
#define IC(n) std::integral_constant<int, (n)>{}
#define RLX_AGENT __ATOMIC_RELAXED, __HIP_MEMORY_SCOPE_AGENT

template <class F, int... I> DEVI void sfor_impl(F&& f, std::integer_sequence<int, I...>) { (f(std::integral_constant<int, I>{}), ...); }
template <int N, class F> DEVI void sfor(F&& f) { sfor_impl(f, std::make_integer_sequence<int, N>{}); }

constexpr size_t WS_H   = 0;
constexpr size_t WS_PQ  = WS_H + 128ull * 48 * 1024;
constexpr size_t WS_KV  = WS_PQ + 128ull * 20 * 1024;
constexpr size_t WS_ZP  = WS_KV + 32ull * 16 * 1600;
constexpr size_t WS_M   = WS_ZP + 16ull * 2560;
constexpr size_t WS_FP  = WS_M + 8ull * 100 * 1024;
constexpr size_t WS_FK  = WS_FP + 32768;
constexpr size_t WS_FE  = WS_FK + 16384;
constexpr size_t WS_EP  = WS_FE + 32768;
constexpr size_t WS_WD  = WS_EP + 1024;
constexpr size_t WS_DUM = WS_WD + 8ull * 240 * 1024;

DEVI v8bf cat8(v4bf a, v4bf b) { return __builtin_shufflevector(a, b, 0, 1, 2, 3, 4, 5, 6, 7); }
DEVI v4bf trd(unsigned addr) { return __builtin_amdgcn_ds_read_tr16_b64_v4bf16((LAS v4bf*)addr); }
DEVI v8bf ldsr16(unsigned addr) { return *(LAS v8bf*)addr; }
DEVI f32x4 mfma16(v8bf a, v8bf b, f32x4 c) { return __builtin_amdgcn_mfma_f32_16x16x32_bf16(a, b, c, 0, 0, 0); }
DEVI f32x4 mfma16u(u32x4 a, u32x4 b, f32x4 c) { return __builtin_amdgcn_mfma_f32_16x16x32_bf16(__builtin_bit_cast(v8bf, a), __builtin_bit_cast(v8bf, b), c, 0, 0, 0); }
DEVI void mfma_a(f32x4& c, v8bf a, v8bf b) { asm volatile("v_mfma_f32_16x16x32_bf16 %0, %1, %2, %0" : "+v"(c) : "v"(a), "v"(b)); }
DEVI void mfma_au(f32x4& c, u32x4 a, u32x4 b) { asm volatile("v_mfma_f32_16x16x32_bf16 %0, %1, %2, %0" : "+v"(c) : "v"(a), "v"(b)); }
DEVI unsigned pk2(float lo, float hi) { v2bf v = {(bf16)lo, (bf16)hi}; return __builtin_bit_cast(unsigned, v); }
DEVI float blo(unsigned d) { return __uint_as_float(d << 16); }
DEVI float bhi(unsigned d) { return __uint_as_float(d & 0xffff0000u); }

DEVI void glds_full(unsigned ldsdst, unsigned voff, const void* sbase) {
  asm volatile("s_mov_b32 m0, %0\n\ts_nop 0\n\tglobal_load_lds_dwordx4 %1, %2" ::"s"(ldsdst), "v"(voff), "s"(sbase) : "memory");
}
DEVI void glds_sc1(unsigned ldsdst, unsigned voff, const void* sbase) {
  asm volatile("s_mov_b32 m0, %0\n\ts_nop 0\n\tglobal_load_lds_dwordx4 %1, %2 sc1" ::"s"(ldsdst), "v"(voff), "s"(sbase) : "memory");
}
DEVI void glds_half(unsigned ldsdst, unsigned voff, const void* sbase) {
  asm volatile("s_mov_b32 m0, %0\n\ts_mov_b32 exec_hi, 0\n\tglobal_load_lds_dwordx4 %1, %2\n\ts_mov_b32 exec_hi, -1" ::"s"(ldsdst), "v"(voff), "s"(sbase) : "memory");
}
template <int N> DEVI void wait_vm() { asm volatile("s_waitcnt vmcnt(%0)" ::"n"(N) : "memory"); }
DEVI void wait_lgkm0() { asm volatile("s_waitcnt lgkmcnt(0)" ::: "memory"); }
DEVI void bar() { __builtin_amdgcn_s_barrier(); }
DEVI void gld16_sc1(u32x4& d, unsigned voff, const void* sbase) {
  asm volatile("global_load_dwordx4 %0, %1, %2 sc1" : "=v"(d) : "v"(voff), "s"(sbase) : "memory");
}
template <int N> DEVI void wait_vm4(u32x4& a, u32x4& b, u32x4& c, u32x4& d) {
  asm volatile("s_waitcnt vmcnt(%4)" : "+v"(a), "+v"(b), "+v"(c), "+v"(d) : "n"(N) : "memory");
}
DEVI void pin4(u32x4& a, u32x4& b, u32x4& c, u32x4& d) { asm volatile("" : "+v"(a), "+v"(b), "+v"(c), "+v"(d)); }
DEVI void pin1(u32x4& a) { asm volatile("" : "+v"(a)); }
DEVI void st16(void* p, u32x4 v) { asm volatile("global_store_dwordx4 %0, %1, off sc1\n\ts_nop 1" ::"v"(p), "v"(v) : "memory"); }
DEVI void st16f(void* p, f32x4 v) { asm volatile("global_store_dwordx4 %0, %1, off sc1\n\ts_nop 1" ::"v"(p), "v"(v) : "memory"); }
DEVI void st8(void* p, u32x2 v) { asm volatile("global_store_dwordx2 %0, %1, off sc1" ::"v"(p), "v"(v) : "memory"); }
DEVI void st4(void* p, float v) { asm volatile("global_store_dword %0, %1, off sc1" ::"v"(p), "v"(v) : "memory"); }
DEVI float xsum4(float x) {
  auto a = __builtin_amdgcn_permlane32_swap(__float_as_uint(x), __float_as_uint(x), false, false);
  unsigned a0 = a[0], a1 = a[1];
  float y = __uint_as_float(a0) + __uint_as_float(a1);
  auto b = __builtin_amdgcn_permlane16_swap(__float_as_uint(y), __float_as_uint(y), false, false);
  unsigned b0 = b[0], b1 = b[1];
  return __uint_as_float(b0) + __uint_as_float(b1);
}
#define PSLEEP 6
DEVI void wait_flags(const char* p, u64 tag) {
  for (unsigned spins = 0; spins < (1u << 20); ++spins) {
    u64 v = __hip_atomic_load((GAS u64*)p, RLX_AGENT);
    if (__all(v == tag)) break;
    __builtin_amdgcn_s_sleep(PSLEEP);
  }
  asm volatile("" ::: "memory");
}

DEVI void wait_lds(unsigned addr, unsigned mask) {
  for (unsigned spins = 0; spins < (1u << 24); ++spins) {
    unsigned v = __builtin_amdgcn_readfirstlane(*(volatile LAS unsigned*)addr);
    if ((v & mask) == mask) break;
    __builtin_amdgcn_s_sleep(1);
  }
  asm volatile("" ::: "memory");
}

#define ST(i)

DEVI float exp2_clamp(float t) { float e; asm("v_exp_f32_e64 %0, %1 clamp" : "=v"(e) : "v"(t)); return e; }
DEVI unsigned phi_pk(f32x2 t2) {
  f32x2 a = t2 * 1.4426950408889634f;
  f32x2 e = {exp2_clamp(a[0]), exp2_clamp(a[1])};
  f32x2 m = {__builtin_fmaxf(t2[0], 1e-6f), __builtin_fmaxf(t2[1], 1e-6f)};
  f32x2 o = m + e;
  return pk2(o[0], o[1]);
}
DEVI unsigned geglu_pk(f32x2 g, f32x2 u, float c1, float c3, float ia2) {
  f32x2 t = g * (g * g * c3 + c1);
  f32x2 e = {__builtin_amdgcn_exp2f(t[0]), __builtin_amdgcn_exp2f(t[1])};
  f32x2 d = e * ia2 + ia2;
  f32x2 rr = {__builtin_amdgcn_rcpf(d[0]), __builtin_amdgcn_rcpf(d[1])};
  f32x2 o = (g * u) * rr;
  return pk2(o[0], o[1]);
}
DEVI f32x2 lo2(f32x4 v) { return (f32x2){v[0], v[1]}; }
DEVI f32x2 hi2(f32x4 v) { return (f32x2){v[2], v[3]}; }

constexpr int WSLOT = 20480, NSW = 5, R1OFF = 10240;
constexpr int XOFF = NSW * WSLOT, XSLOT = 16384;
constexpr int STOFF = XOFF + 3 * XSLOT;
constexpr int ST_RS = STOFF;
constexpr int ST_CS = STOFF + 1024;
constexpr int ST_C4 = ST_CS + 1024;
constexpr int ST_ONE = ST_C4 + 512;
constexpr int ST_SINK = ST_ONE + 64;
constexpr int KVP = 328;
constexpr int KVI0 = 61440;
constexpr int KVI1 = 128 * KVP;
constexpr int SLOT = 5120;
constexpr int P2_SCR = 28 * SLOT;
constexpr int P2_FLG = P2_SCR + 16 * 1024;
constexpr int LDS_BYTES = 163840;

__global__ __launch_bounds__(512, 2) void fused_kernel(const bf16* __restrict__ x, const bf16* __restrict__ wqkv,
                                                       const bf16* __restrict__ wo, const bf16* __restrict__ wg,
                                                       const bf16* __restrict__ wu, const bf16* __restrict__ wdn,
                                                       char* __restrict__ ws, bf16* __restrict__ out) {
  __shared__ __attribute__((aligned(16))) char smem[LDS_BYTES];
  const unsigned lb = (unsigned)(uintptr_t)(LAS char*)smem;
  const int tid = threadIdx.x, lane = tid & 63;
  const int w = __builtin_amdgcn_readfirstlane(tid >> 6);
  const int tw = w >> 2, p = w & 3;
  const int r = lane & 15, g4 = lane >> 4, q = (lane & 15) >> 2, pp = lane & 3;
  const int bid = blockIdx.x, xcd = bid & 7, idx = bid >> 3;
  const int mt = 8 * (xcd >> 2) + (idx & 7);
  const int nt = (int)((0xDCB3A982F751E640ull >> (16 * (xcd & 3) + 4 * (idx >> 3))) & 15);
  const int mt2 = 8 * (xcd >> 1) + (idx >> 2), c = 4 * (xcd & 1) + (idx & 3);
  ST(0);

  u32x2 epv;
  asm volatile("global_load_dwordx2 %0, %1, %2 sc1" : "=v"(epv) : "v"(0u), "s"(ws + WS_EP) : "memory");

  f32x4 acc[4][5];
  const unsigned l16 = lane * 16;
  u64 tag;
  {
  const bf16 *r0, *r1; int ld;
  if (nt < 4) { r0 = wqkv + 640 + 160 * nt; r1 = wqkv + 1280 + 160 * nt; ld = 1920; }
  else if (nt < 6) { r0 = wqkv + 320 * (nt - 4); r1 = r0 + 160; ld = 1920; }
  else { int t = nt - 6; int hb = t < 8 ? 160 * t : 1280 + 128 * (t - 8); r0 = wg + hb; r1 = wu + hb; ld = 1536; }
  const bool last_short = (nt == 15);

  unsigned woff[3];
#pragma unroll
  for (int i = 0; i < 3; ++i) {
    int L = 64 * i + lane; if (L > 159) L = 159;
    int row = L / 20, ch = L - row * 20;
    ch ^= ((w & 1) << 1) | (w >> 2);
    if (last_short && ch >= 16) ch -= 4;
    woff[i] = (unsigned)(row * ld * 2 + ch * 16);
  }
  const unsigned xoff = (unsigned)((lane >> 3) * 1280 + (((lane & 7) ^ ((4 * w + (lane >> 4)) & 7)) * 16));
  const char* wptr = (const char*)((w >> 2) ? r1 : r0) + (size_t)(8 * (w & 3)) * ld * 2;
  const unsigned wstep = 32u * ld * 2;
  const char* xptr = (const char*)x + (size_t)(128 * mt + 8 * w) * 1280;
  const unsigned wdst = lb + (w >> 2) * R1OFF + (w & 3) * 2560;
  const unsigned xdst = lb + XOFF + w * 1024;

#define ROT 3
  const int rot = ROT == 0 ? 0 : (5 * (xcd >> 2) + ((0x7520 >> (4 * (xcd & 3))) & 15) + (ROT == 2 ? 3 * (idx & 1) : (ROT == 3 ? (idx & 3) + ((idx & 3) >> 1 & (idx & 1)) : (ROT == 4 ? (idx & 7) + 3 * (idx >> 3) : 0)))) % 10;
  auto issue_w = [&](int st, int i) {
    int kk = st + 2 * rot; kk = kk >= 20 ? kk - 20 : kk;
    unsigned so = (unsigned)kk * wstep; asm("" : "+s"(so));
    const char* b = wptr + so;
    unsigned d = wdst + (st % NSW) * WSLOT + i * 1024;
    if (i < 2) glds_full(d, woff[i], b); else glds_half(d, woff[i], b);
  };
  auto issue_x = [&](int J, int h) {
    int kk = J + rot; kk = kk >= 10 ? kk - 10 : kk;
    glds_full(xdst + (J % 3) * XSLOT + h * 8192, xoff, xptr + h * 64 * 1280 + kk * 128);
  };
  auto issue_W = [&](int st) { issue_w(st, 0); issue_w(st, 1); issue_w(st, 2); };
  auto issue_X = [&](int J) { issue_x(J, 0); issue_x(J, 1); };
  issue_X(0); issue_W(0); issue_W(1);
  {
    u64 pc; unsigned len;
    asm volatile("s_getpc_b64 %0\n.Lfk_begin:\n\ts_mov_b32 %1, .Lfk_end-.Lfk_begin" : "=s"(pc), "=s"(len));
    unsigned off = (unsigned)((idx * 8 + w) * 2 + (lane & 1)) * 128; off = off < len ? off : 0;
    asm volatile("s_mov_b32 m0, %0\n\ts_nop 0\n\tglobal_load_lds_dword %1, %2" ::"s"(lb + ST_SINK + w * 256), "v"(off), "s"(pc) : "memory");
  }
  constexpr int NCPF = 1;
  issue_X(1); issue_W(2); issue_W(3);
  if (tid < 8) *(LAS unsigned*)(lb + P2_FLG + tid * 4) = 0u;

  const unsigned rowb = (8 * g4 + q) * 320 + (pp & 1) * 8;
  const int sw = (g4 & 1) << 1;
  unsigned wa[5][2];
  wa[0][0] = lb + rowb + ((4 * p + 2 * tw + (pp >> 1)) ^ sw) * 16;
  wa[1][0] = lb + rowb + R1OFF + ((4 * p + 2 * tw + (pp >> 1)) ^ sw ^ 1) * 16;
  wa[2][0] = lb + rowb + ((4 * p + 2 * (1 - tw) + (pp >> 1)) ^ sw) * 16;
  wa[3][0] = lb + rowb + R1OFF + ((4 * p + 2 * (1 - tw) + (pp >> 1)) ^ sw ^ 1) * 16;
  wa[4][0] = lb + rowb + (pp >> 1) * R1OFF + ((16 + p) ^ sw ^ (pp >> 1)) * 16;
#pragma unroll
  for (int i = 0; i < 5; ++i) { wa[i][1] = wa[i][0] + 3 * WSLOT; asm volatile("" : "+v"(wa[i][1])); }
  unsigned xa[4][2];
#pragma unroll
  for (int m = 0; m < 4; ++m) {
    unsigned t = lb + XOFF + (64 * tw + 16 * (m ^ p) + r) * 128;
    xa[m][0] = t + ((g4 ^ ((r >> 1) & 7)) * 16);
    xa[m][1] = t + (((4 + g4) ^ ((r >> 1) & 7)) * 16);
  }
  if (tid < 16) *(LAS unsigned*)(lb + ST_ONE + tid * 4) = 0x3f803f80u;

#pragma unroll
  for (int m = 0; m < 4; ++m)
#pragma unroll
    for (int f = 0; f < 5; ++f) acc[m][f] = (f32x4){0.f, 0.f, 0.f, 0.f};
  f32x4 cs0 = {0.f, 0.f, 0.f, 0.f}, cs1 = {0.f, 0.f, 0.f, 0.f};
  float s0 = 0.f, s1 = 0.f, q0 = 0.f, q1 = 0.f, c40 = 0.f, c41 = 0.f;
  const bf16 one = (bf16)1.0f;
  v8bf ones = {one, one, one, one, one, one, one, one};
  asm volatile("" : "+v"(ones), "+v"(cs0), "+v"(cs1));
#pragma unroll
  for (int m = 0; m < 4; ++m) asm volatile("" : "+v"(acc[m][0]), "+v"(acc[m][1]), "+v"(acc[m][2]), "+v"(acc[m][3]), "+v"(acc[m][4]));
  const unsigned selLo = __builtin_amdgcn_readfirstlane(tw ? 0x05040c0cu : 0x01000c0cu), selHi = __builtin_amdgcn_readfirstlane(tw ? 0x07060c0cu : 0x03020c0cu);

  const char* b1 = ws + WS_WD + (size_t)((c * 48 + 12 * p + tw) * 5) * 1024;
  const char* b2 = ws + WS_WD + (size_t)((c * 48 + 12 * p + 2) * 5) * 1024;
  const int fl = mt2 * 8 + w, fks = (fl < 240 ? fl : 0) / 5, ff = (fl < 240 ? fl : 0) % 5;
  u32x4 wd0, wd1; u32x2 wfl;
  const char* fep = ws + WS_FE + (size_t)((idx & 15) * 256 + w * 32) * 8;
  v4bf Wh[2][5][2]; v8bf Xf[2][4];
  auto rdW = [&](auto SC, auto FC, auto TC) {
    constexpr int s = SC, f = FC, t = TC; constexpr int b = s & 1;
    constexpr int sl = s % NSW;
    constexpr unsigned wo_ = (sl >= 3 ? sl - 3 : sl) * WSLOT + t * 1280;
    Wh[b][f][t] = trd(wa[f][sl >= 3] + wo_);
  };
  auto rdX = [&](auto SC, auto MC) {
    constexpr int s = SC, m = MC; constexpr int b = s & 1;
    Xf[b][m] = ldsr16(xa[m][s & 1] + ((s >> 1) % 3) * XSLOT);
  };
  auto rdI = [&](auto SC, auto IC_) {
    constexpr int i = IC_;
    if constexpr (i < 2) rdW(SC, IC(0), IC(i));
    else if constexpr (i < 6) rdX(SC, IC(i - 2));
    else rdW(SC, IC((i - 4) >> 1), IC(i & 1));
  };
  asm volatile("s_waitcnt vmcnt(%1)" : "+v"(epv) : "n"(8 + NCPF) : "memory");
  bar();
  ST(1);
  tag = (((u64)__builtin_amdgcn_readfirstlane(epv[1]) << 32) | (u64)(unsigned)__builtin_amdgcn_readfirstlane(epv[0])) + 0x9E3779B97F4A7C15ull;
  sfor<14>([&](auto I) { rdI(IC(0), I); });

  sfor<20>([&](auto SC) {
    constexpr int s = SC; constexpr int b = s & 1;
    constexpr bool more = s < 19;
    if constexpr ((s & 1) && more) {
      SB;
      if constexpr (s == 3) asm volatile("s_waitcnt vmcnt(5)" : "+v"(wd0), "+v"(wd1) :: "memory");
      else if constexpr (s == 17) {
        asm volatile("s_waitcnt vmcnt(0)" : "+v"(wfl) :: "memory");
        const u64 got = ((u64)wfl[1] << 32) | wfl[0];
        if (!__all(got == tag)) wait_flags(fep + (lane & 31) * 8, tag);
      } else wait_vm<5>();
      wait_lgkm0();
      bar();
      if constexpr (s == 1) {
        const char* src = (const char*)wdn + (size_t)(32 * fks) * 1280 + c * 160 + ff * 32;
        const unsigned o = (4 * g4) * 1280 + r * 2;
        asm volatile("global_load_ushort %0, %8, %9\n\tglobal_load_ushort %1, %8, %9 offset:1280\n\t"
                     "global_load_ushort %2, %8, %9 offset:2560\n\tglobal_load_ushort %3, %8, %9 offset:3840\n\t"
                     "global_load_ushort %4, %8, %10\n\tglobal_load_ushort %5, %8, %10 offset:1280\n\t"
                     "global_load_ushort %6, %8, %10 offset:2560\n\tglobal_load_ushort %7, %8, %10 offset:3840"
                     : "=&v"(wd0[0]), "=&v"(wd0[1]), "=&v"(wd0[2]), "=&v"(wd0[3]), "=&v"(wd1[0]), "=&v"(wd1[1]), "=&v"(wd1[2]), "=&v"(wd1[3])
                     : "v"(o), "s"(src), "s"(src + 16 * 1280) : "memory");
      }
      if constexpr (s == 3) {
        u32x4 wdv = {wd0[0] | (wd0[1] << 16), wd0[2] | (wd0[3] << 16), wd1[0] | (wd1[1] << 16), wd1[2] | (wd1[3] << 16)};
        const size_t fo = fl < 240 ? WS_WD + (size_t)(c * 240 + fl) * 1024 : WS_DUM + (size_t)(c * 16 + fl - 240) * 1024;
        asm volatile("global_store_dwordx4 %0, %1, %2 sc1\n\ts_nop 1" ::"v"(l16), "v"(wdv), "s"(ws + fo) : "memory");
      }
      if constexpr (s == 3) {
        const int gw = bid * 8 + w;
        const size_t fo = gw < 1640 ? WS_KV + (size_t)gw * 1024 : WS_DUM + 131072 + (size_t)(gw - 1640) * 1024;
        const u32x4 pz = {~0u, ~0u, ~0u, ~0u};
        asm volatile("global_store_dwordx4 %0, %1, %2 sc1\n\ts_nop 1" ::"v"(l16), "v"(pz), "s"(ws + fo) : "memory");
      }
      if constexpr (s == 5) {
        const unsigned fo = tid < 16 ? (unsigned)(WS_FE + (tid * 256 + bid) * 8) : (unsigned)(WS_DUM + 655360 + (bid * 8 + w) * 128);
        asm volatile("global_store_dwordx2 %0, %1, %2 sc1" ::"v"(fo), "v"(tag), "s"(ws) : "memory");
      }
      if constexpr (s == 13) asm volatile("global_load_dwordx2 %0, %1, %2 sc1" : "=v"(wfl) : "v"((unsigned)(lane & 31) * 8), "s"(fep) : "memory");
      if constexpr (s == 17) {
        const unsigned fo = (bid == 0 && tid == 0) ? (unsigned)WS_EP : (unsigned)(WS_DUM + 655360 + (bid * 8 + w) * 128);
        asm volatile("global_store_dwordx2 %0, %1, %2 sc1" ::"v"(fo), "v"(tag), "s"(ws) : "memory");
      }
    }
    SB;
    u32x4 xv = __builtin_bit_cast(u32x4, Xf[b][0]);
    u32x2 w4a = __builtin_bit_cast(u32x2, Wh[b][4][0]), w4b = __builtin_bit_cast(u32x2, Wh[b][4][1]);
    float lo, hi;
    sfor<22>([&](auto KC) {
      constexpr int k = KC;
      if constexpr (k == 4) mfma_a(cs0, cat8(Wh[b][0][0], Wh[b][0][1]), ones);
      else if constexpr (k == 9) mfma_a(cs1, cat8(Wh[b][1][0], Wh[b][1][1]), ones);
      else {
        constexpr int kk = k - (k > 4) - (k > 9), f = kk >> 2, m = kk & 3;
        mfma_a(acc[m][f], cat8(Wh[b][f][0], Wh[b][f][1]), Xf[b][m]);
      }
      SB;
      if constexpr (more && k < 14) rdI(IC(s + 1), KC);
      if constexpr (k < 12) {
        constexpr int j = k / 3, ph = k % 3;
        unsigned xj = xv[j];
        if constexpr (ph == 0) asm volatile("v_lshlrev_b32 %0, 16, %2\n\tv_and_b32 %1, 0xffff0000, %2" : "=&v"(lo), "=v"(hi) : "v"(xj));
        if constexpr (ph == 1) asm volatile("v_add_f32 %0, %0, %2\n\tv_add_f32 %1, %1, %3" : "+v"(s0), "+v"(s1) : "v"(lo), "v"(hi));
        if constexpr (ph == 2) asm volatile("v_fmac_f32 %0, %2, %2\n\tv_fmac_f32 %1, %3, %3" : "+v"(q0), "+v"(q1) : "v"(lo), "v"(hi));
      } else if constexpr (k == 13 || k == 15) {
        unsigned a0 = w4a[k == 15], b0 = w4b[k == 15];
        asm volatile("v_perm_b32 %0, %2, %3, %4\n\tv_perm_b32 %1, %2, %3, %5" : "=&v"(lo), "=v"(hi) : "v"(b0), "v"(a0), "s"(selLo), "s"(selHi));
      } else if constexpr (k == 14 || k == 16) {
        asm volatile("v_add_f32 %0, %0, %2\n\tv_add_f32 %1, %1, %3" : "+v"(c40), "+v"(c41) : "v"(lo), "v"(hi));
      }
      if constexpr (s >= 17 && (k & 3) == 3 && (s - 17) * 5 + (k >> 2) < 8) {
        constexpr int i = (s - 17) * 5 + (k >> 2);
        if constexpr (i < 5) glds_sc1(lb + w * SLOT + i * 1024, l16, b1 + i * 1024);
        else {
          constexpr int i0 = i - 5, i1 = i0 == 0 ? 3 : 4;
          glds_sc1(lb + (8 + p) * SLOT + (tw ? i1 : i0) * 1024, l16, b2 + (tw ? i1 : i0) * 1024);
        }
      }
      if constexpr (k >= 12 && !(k & 1)) {
        constexpr int f = (k - 14) / 2;
        if constexpr (s == 0) {
          if constexpr (f + 1 < 3) issue_w(4, f + 1); else issue_x(2, f + 1 - 3);
        } else if constexpr (f >= 0) {
          if constexpr (s & 1) {
            if constexpr (f < 3 && s + 4 <= 19) issue_w(s + 4, f);
            if constexpr (f == 3 && s + 5 <= 19) issue_w(s + 5, 0);
          } else {
            if constexpr (f < 2 && s + 4 <= 19) issue_w(s + 4, f + 1);
            if constexpr (f >= 2 && (s + 4) / 2 <= 9) issue_x((s + 4) / 2, f - 2);
          }
        }
      }
      SB;
    });
  });
  asm volatile("s_nop 15" ::: "memory");
  SB;
  ST(2);
  {
    float s = xsum4(s0 + s1), qq = xsum4(q0 + q1), c4 = xsum4(c40 + c41);
    float mean = s * (1.0f / 640.0f);
    float ve = __builtin_fmaxf(qq * (1.0f / 640.0f) - mean * mean, 0.f) + 1e-5f;
    if (g4 == 0) {
      *(LAS f32x2*)(lb + ST_RS + (64 * tw + 16 * p + r) * 8) = (f32x2){ve, -mean};
      *(LAS float*)(lb + ST_C4 + ((tw * 4 + p) * 16 + r) * 4) = c4;
    }
    if (r == 0) {
      *(LAS f32x4*)(lb + ST_CS + ((p * 4 + 2 * tw) * 16 + 4 * g4) * 4) = cs0;
      *(LAS f32x4*)(lb + ST_CS + ((p * 4 + 2 * tw + 1) * 16 + 4 * g4) * 4) = cs1;
    }
  }
  wait_lgkm0();
  bar();
  f32x4 csv[5];
  csv[0] = cs0; csv[1] = cs1;
  csv[2] = *(LAS f32x4*)(lb + ST_CS + ((p * 4 + 2 * (1 - tw)) * 16 + 4 * g4) * 4);
  csv[3] = *(LAS f32x4*)(lb + ST_CS + ((p * 4 + 2 * (1 - tw) + 1) * 16 + 4 * g4) * 4);
  csv[4] = *(LAS f32x4*)(lb + ST_C4 + (p * 16 + 4 * g4) * 4) + *(LAS f32x4*)(lb + ST_C4 + ((4 + p) * 16 + 4 * g4) * 4);
  float ve[4], ra[4];
#pragma unroll
  for (int m = 0; m < 4; ++m) {
    f32x2 rs = *(LAS f32x2*)(lb + ST_RS + (64 * tw + 16 * (m ^ p) + r) * 8);
    ve[m] = rs[0]; ra[m] = __builtin_amdgcn_rsqf(rs[0]);
#pragma unroll
    for (int f = 0; f < 5; ++f) acc[m][f] += csv[f] * rs[1];
  }
  if (nt >= 4) {
    const char* b3 = ws + WS_WD + (size_t)((c * 48 + 12 * p + 3 + 2 * tw) * 5) * 1024;
#pragma unroll
    for (int i = 0; i < 10; ++i) glds_sc1(lb + (12 + 8 * tw + 4 * (i / 5) + p) * SLOT + (i % 5) * 1024, l16, b3 + i * 1024);
  }
  ST(3);

  if (nt >= 6) {
    const int t = nt - 6;
    const int ksb = t < 8 ? 5 * t : 40 + 4 * (t - 8);
    char* hb = ws + WS_H;
    const float k1 = -2.0f * 0.7978845608028654f * 1.4426950408889634f, k3 = k1 * 0.044715f;
    float c1[4], c3[4];
#pragma unroll
    for (int m = 0; m < 4; ++m) {
      c1[m] = ra[m] * k1; c3[m] = ra[m] * ra[m] * ra[m] * k3;
      unsigned a0 = geglu_pk(lo2(acc[m][0]), lo2(acc[m][1]), c1[m], c3[m], ve[m]);
      unsigned a1 = geglu_pk(hi2(acc[m][0]), hi2(acc[m][1]), c1[m], c3[m], ve[m]);
      unsigned b0 = geglu_pk(lo2(acc[m][2]), lo2(acc[m][3]), c1[m], c3[m], ve[m]);
      unsigned b1 = geglu_pk(hi2(acc[m][2]), hi2(acc[m][3]), c1[m], c3[m], ve[m]);
      int mf = 8 * mt + 4 * tw + (m ^ p);
      char* dst = hb + ((size_t)(mf * 48 + ksb + p) * 64 + lane) * 16;
      if (tw) st16(dst, (u32x4){b0, b1, a0, a1}); else st16(dst, (u32x4){a0, a1, b0, b1});
    }
    if (t < 8) {
#pragma unroll
      for (int m = 0; m < 4; m += 2) {
        f32x4 gv, uv;
#pragma unroll
        for (int j = 0; j < 4; ++j) {
          float fa = acc[m][4][j], fb = acc[m + 1][4][j];
          auto sw2 = __builtin_amdgcn_permlane32_swap(__float_as_uint(fa), __float_as_uint(fb), false, false);
          unsigned ua = sw2[0], ub = sw2[1];
          gv[j] = __uint_as_float(ua); uv[j] = __uint_as_float(ub);
        }
        const bool hi = g4 >= 2;
        const float d1 = hi ? c1[m + 1] : c1[m], d3 = hi ? c3[m + 1] : c3[m], dv = hi ? ve[m + 1] : ve[m];
        u32x2 o = {geglu_pk(lo2(gv), lo2(uv), d1, d3, dv), geglu_pk(hi2(gv), hi2(uv), d1, d3, dv)};
        int mf = 8 * mt + 4 * tw + ((m + (g4 >> 1)) ^ p);
        st8(hb + ((size_t)(mf * 48 + ksb + 4) * 64 + r + 16 * (2 * (p & 1) + (g4 & 1))) * 16 + (p >> 1) * 8, o);
      }
    }
  } else if (nt >= 4) {
    const int ksb = 10 * (nt - 4);
    char* qb = ws + WS_PQ;
#pragma unroll
    for (int m = 0; m < 4; ++m) {
      unsigned e[5][2];
#pragma unroll
      for (int f = 0; f < 5; ++f) {
        f32x4 t2 = acc[m][f] * ra[m] + 1e-6f;
        e[f][0] = phi_pk(lo2(t2)); e[f][1] = phi_pk(hi2(t2));
      }
      int mf = 8 * mt + 4 * tw + (m ^ p);
      char* d0 = qb + ((size_t)(mf * 20 + ksb + p) * 64 + lane) * 16;
      if (tw) { st16(d0, (u32x4){e[2][0], e[2][1], e[0][0], e[0][1]}); st16(d0 + 5120, (u32x4){e[3][0], e[3][1], e[1][0], e[1][1]}); }
      else    { st16(d0, (u32x4){e[0][0], e[0][1], e[2][0], e[2][1]}); st16(d0 + 5120, (u32x4){e[1][0], e[1][1], e[3][0], e[3][1]}); }
      st8(qb + ((size_t)(mf * 20 + ksb + 4 + 5 * (g4 >> 1)) * 64 + r + 16 * (2 * (p & 1) + (g4 & 1))) * 16 + (p >> 1) * 8, (u32x2){e[4][0], e[4][1]});
    }
  } else {
#pragma unroll
    for (int m = 0; m < 4; ++m) {
      unsigned rowo = lb + KVI0 + (64 * tw + 16 * (m ^ p) + r) * KVP + 8 * g4;
      unsigned cA = 64 * p + 32 * tw, cB = 64 * p + 32 * (1 - tw);
      f32x4 k0 = acc[m][0] * ra[m] + 1e-6f, k2 = acc[m][2] * ra[m] + 1e-6f, v1 = acc[m][1] * ra[m], v3 = acc[m][3] * ra[m];
      f32x4 x4 = acc[m][4] * ra[m], k4 = x4 + 1e-6f;
      *(LAS u32x2*)(rowo + cA) = (u32x2){phi_pk(lo2(k0)), phi_pk(hi2(k0))};
      *(LAS u32x2*)(rowo + cB) = (u32x2){phi_pk(lo2(k2)), phi_pk(hi2(k2))};
      *(LAS u32x2*)(rowo + KVI1 + cA) = (u32x2){pk2(v1[0], v1[1]), pk2(v1[2], v1[3])};
      *(LAS u32x2*)(rowo + KVI1 + cB) = (u32x2){pk2(v3[0], v3[1]), pk2(v3[2], v3[3])};
      unsigned p0 = phi_pk(lo2(k4)), p1 = phi_pk(hi2(k4)), w0 = pk2(x4[0], x4[1]), w1 = pk2(x4[2], x4[3]);
      asm("" : "+v"(p0), "+v"(p1));
      unsigned o4 = lb + KVI0 + (64 * tw + 16 * (m ^ p) + r) * KVP + (g4 >> 1) * KVI1 + 256 + 16 * p + 8 * (g4 & 1);
      *(LAS u32x2*)(o4) = (u32x2){g4 < 2 ? p0 : w0, g4 < 2 ? p1 : w1};
    }
    wait_vm<0>();
    wait_lgkm0();
    bar();
    f32x4 t00 = {0.f, 0.f, 0.f, 0.f}, t01 = t00, t10 = t00, t11 = t00;
    const unsigned tb = lb + KVI0 + (4 * g4 + q) * KVP + 40 * w;
    const unsigned kLo = tb + pp * 8, kHi = tb + 32;
    const unsigned vLo = tb + KVI1 + pp * 8;
    const unsigned vHiA = tb + KVI1 + 32, onesA = lb + ST_ONE;
#pragma unroll
    for (int ts = 0; ts < 4; ++ts) {
      unsigned o0 = ts * 32 * KVP, o1 = o0 + 16 * KVP;
      v8bf kl = cat8(trd(kLo + o0), trd(kLo + o1));
      v8bf kh = cat8(trd(kHi + o0), trd(kHi + o1));
      v8bf vl = cat8(trd(vLo + o0), trd(vLo + o1));
      v8bf vh = cat8(trd(pp == 0 ? vHiA + o0 : onesA), trd(pp == 0 ? vHiA + o1 : onesA));
      t00 = mfma16(vl, kl, t00); t01 = mfma16(vl, kh, t01);
      t10 = mfma16(vh, kl, t10); t11 = mfma16(vh, kh, t11);
    }
    asm volatile("s_nop 7\n\ts_nop 7" : "+v"(t00), "+v"(t01), "+v"(t10), "+v"(t11));
    const int hd = 8 * nt + w;
    float* kvp = (float*)(ws + WS_KV) + (size_t)(hd * 16 + mt) * 400;
    float* zp = (float*)(ws + WS_ZP) + mt * 640 + hd * 20;
    st16f(kvp + r * 20 + 4 * g4, t00);
    if (g4 == 0) st16f(kvp + r * 20 + 16, t10);
    if (g4 == 1) st4(zp + r, t10[0]);
    if (r < 4) {
      st16f(kvp + (16 + r) * 20 + 4 * g4, t01);
      if (g4 == 0) st16f(kvp + (16 + r) * 20 + 16, t11);
      if (g4 == 1) st4(zp + 16 + r, t11[0]);
    }
  }
  }
  ST(4);
  if (nt >= 4) wait_vm<0>();
  wait_lgkm0();
  bar();
  if (tid < (nt < 4 ? 32 : 16))
    __hip_atomic_store((GAS u64*)(ws + (nt < 4 ? WS_FK + (size_t)((tid * 4 + nt) * 16 + mt) * 8 : WS_FP + (size_t)((mt * 16 + tid) * 16 + nt) * 8)), tag, RLX_AGENT);
  const char* fpan = ws + WS_FP + (size_t)(((mt2 >> 1) * 16 + (mt2 & 1) * 8 + c) * 16) * 8;
  const char* fkv = ws + WS_FK + (size_t)idx * 512;
  ST(5);

  auto zero_acc = [&]() {
#pragma unroll
    for (int m = 0; m < 4; ++m) {
#pragma unroll
      for (int f = 0; f < 5; ++f) acc[m][f] = (f32x4){0.f, 0.f, 0.f, 0.f};
      asm volatile("" : "+v"(acc[m][0]), "+v"(acc[m][1]), "+v"(acc[m][2]), "+v"(acc[m][3]), "+v"(acc[m][4]));
    }
    asm volatile("s_nop 1" ::: "memory");
  };
  const unsigned trb = (4 * g4 + q) * 160 + pp * 8;
  u32x2 xres[3];
#pragma unroll
  for (int i = 0; i < 3; ++i) {
    int t = w + 8 * i; t = t < 20 ? t : w;
    const int m = t / 5, f = t - 5 * m;
    xres[i] = *(const u32x2*)(x + (size_t)(64 * mt2 + 16 * m + r) * 640 + 80 * c + 16 * f + 4 * g4);
  }

  if (w >= 4) {
    const int j = w - 4;
    const char* bp = ws + WS_WD + (size_t)((c * 48 + 12 * j) * 5) * 1024;
    const char* ap = ws + WS_H + ((size_t)(4 * mt2) * 48 + 12 * j) * 1024;
    u32x4 A[3][4], Br[3][5], Bc[2][5];
    auto issue_a = [&](auto SC) {
      constexpr int s = SC;
#pragma unroll
      for (int m = 0; m < 4; ++m) gld16_sc1(A[s % 3][m], l16, ap + (size_t)(m * 48 + s) * 1024);
    };
    unsigned rb[3];
    rb[0] = lb + j * SLOT + l16; rb[1] = rb[0] + 12 * SLOT; rb[2] = rb[0] + 24 * SLOT;
    asm volatile("" : "+v"(rb[1]), "+v"(rb[2]));
    auto rdB = [&](u32x4 (&B)[5], int pos) {
#pragma unroll
      for (int f = 0; f < 5; ++f) B[f] = *(LAS u32x4*)(rb[pos / 3] + 4 * (pos % 3) * SLOT + f * 1024);
    };
    if (nt < 4)
    {
#pragma unroll
      for (int s = 3; s < 7; ++s)
#pragma unroll
        for (int f = 0; f < 5; ++f) glds_sc1(lb + (4 * s + j) * SLOT + f * 1024, l16, bp + (s * 5 + f) * 1024);
    }
#pragma unroll
    for (int s = 0; s < 3; ++s)
#pragma unroll
      for (int f = 0; f < 5; ++f) gld16_sc1(Br[s][f], l16, bp + ((7 + s) * 5 + f) * 1024);
    ST(8 + 2);
    wait_lds(lb + P2_FLG + 16, ((0x0380'00F0'001C'0007ull >> (16 * j)) & 0x3ff) << 6);
    ST(8 + 1);
    sfor<3>([&](auto SC) { issue_a(SC); });
    zero_acc();
    wait_vm4<12>(Br[0][0], Br[0][1], Br[0][2], Br[0][3]);
    pin4(Br[0][4], Br[1][0], Br[1][1], Br[1][2]); pin4(Br[1][3], Br[1][4], Br[2][0], Br[2][1]); pin1(Br[2][2]); pin1(Br[2][3]); pin1(Br[2][4]);
    rdB(Bc[0], 0);
    sfor<12>([&](auto SC) {
      constexpr int s = SC; constexpr int b = s % 3;
      constexpr auto gs = [](int g) { return g <= 11 ? (g == 9 ? 14 : 4) : 0; };
      SB;
      wait_vm4<gs(s + 1) + gs(s + 2)>(A[b][0], A[b][1], A[b][2], A[b][3]);
      if constexpr (s >= 10) { pin4(Bc[s & 1][0], Bc[s & 1][1], Bc[s & 1][2], Bc[s & 1][3]); pin1(Bc[s & 1][4]); }
      if constexpr (s + 1 <= 6) rdB(Bc[(s + 1) & 1], s + 1);
      SB;
#pragma unroll
      for (int f = 0; f < 5; ++f)
#pragma unroll
        for (int m = 0; m < 4; ++m) mfma_au(acc[m][f], (s >= 7 && s <= 9) ? Br[(s >= 7 && s <= 9) ? s - 7 : 0][f] : Bc[s & 1][f], A[b][m]);
      SB;
      if constexpr (s == 6) {
        if (lane == 0) *(volatile LAS unsigned*)(lb + P2_FLG + j * 4) = 1u;
#pragma unroll
        for (int f = 0; f < 10; ++f) gld16_sc1(Bc[f / 5][f % 5], l16, bp + (50 + f) * 1024);
      }
      if constexpr (s + 3 < 12) issue_a(IC(s + 3));
    });
    asm volatile("s_nop 15" ::: "memory");
    SB;
    ST(8 + 4);
#pragma unroll
    for (int m = 0; m < 4; ++m)
#pragma unroll
      for (int f = 0; f < 5; ++f) *(LAS f32x4*)(lb + (4 * m + j) * SLOT + f * 1024 + l16) = acc[m][f];
  } else {
    const int wq = w;
    const unsigned my = lb + P2_SCR;
    u32x4 P[5][4];
    const unsigned zs_ = wq == 0 ? my + 1024 : lb + P2_FLG + 64 + (wq - 1) * 320;
    const int h = mt2;
    if (wq == 0) {
#pragma unroll
      for (int i = 0; i < 4; ++i) {
        int L = 64 * i + lane; int row = L / 10, ch = L - row * 10; row = row < 20 ? row : 19;
        glds_full(my + 4096 + i * 1024, (unsigned)(row * 1280 + ch * 16), (const char*)wo + (size_t)(20 * h) * 1280 + c * 160);
      }
      __builtin_amdgcn_s_setprio(3);
      wait_lds(lb + P2_FLG + 16, 1u << (h >> 3));
      ST(8 + 0);
      const char* kp = ws + WS_KV + (size_t)h * 16 * 1600;
      const unsigned l2 = (lane < 36 ? 64 + lane : 64) * 16;
      f32x4 sa, sb;
      for (unsigned spins = 0; spins < (1u << 16); ++spins) {
        u32x4 ka[16], kb[16];
#pragma unroll
        for (int i = 0; i < 16; ++i) { gld16_sc1(ka[i], l16, kp + i * 1600); gld16_sc1(kb[i], l2, kp + i * 1600); }
        wait_vm4<0>(ka[0], ka[1], ka[2], ka[3]);
        pin4(ka[4], ka[5], ka[6], ka[7]); pin4(ka[8], ka[9], ka[10], ka[11]); pin4(ka[12], ka[13], ka[14], ka[15]);
        pin4(kb[0], kb[1], kb[2], kb[3]); pin4(kb[4], kb[5], kb[6], kb[7]); pin4(kb[8], kb[9], kb[10], kb[11]); pin4(kb[12], kb[13], kb[14], kb[15]);
        sa = __builtin_bit_cast(f32x4, ka[0]); sb = __builtin_bit_cast(f32x4, kb[0]);
#pragma unroll
        for (int i = 1; i < 16; ++i) { sa += __builtin_bit_cast(f32x4, ka[i]); sb += __builtin_bit_cast(f32x4, kb[i]); }
        const f32x4 t4 = sa + sb; const float t1 = (t4[0] + t4[1]) + (t4[2] + t4[3]);
        if (!__any(t1 != t1)) break;
        __builtin_amdgcn_s_sleep(4);
      }
      *(LAS u32x2*)(my + 2048 + lane * 8) = (u32x2){pk2(sa[0], sa[1]), pk2(sa[2], sa[3])};
      *(LAS u32x2*)(my + 2048 + 512 + lane * 8) = (u32x2){pk2(sb[0], sb[1]), pk2(sb[2], sb[3])};
      u32x4 Ad[2];
#pragma unroll
      for (int mf = 0; mf < 2; ++mf) {
        int d = mf == 0 ? r : 16 + (r & 3);
        u32x2 lo = *(LAS u32x2*)(my + 2048 + d * 40 + g4 * 8);
        u32x2 hi = *(LAS u32x2*)(my + 2048 + d * 40 + 32);
        Ad[mf] = (u32x4){lo[0], lo[1], g4 == 0 ? hi[0] : 0u, g4 == 0 ? hi[1] : 0u};
      }
      GAS char* mb = (GAS char*)(ws + WS_M) + (size_t)c * 100 * 1024;
      const int cg0 = 5 * h + g4, cg1 = 5 * h + 4;
      const unsigned o0 = (cg0 >> 3) * 5120 + (r + 16 * (cg0 & 3)) * 16 + ((cg0 >> 2) & 1) * 8;
      const unsigned o1 = (cg1 >> 3) * 5120 + (r + 16 * (cg1 & 3)) * 16 + ((cg1 >> 2) & 1) * 8;
#pragma unroll
      for (int f = 0; f < 5; ++f) {
        v8bf B = cat8(trd(my + 4096 + trb + f * 32), trd(my + 4096 + 2560 + q * 160 + pp * 8 + f * 32));
        f32x4 z4 = {0.f, 0.f, 0.f, 0.f};
        f32x4 d0 = mfma16(__builtin_bit_cast(v8bf, Ad[0]), B, z4), d1 = mfma16(__builtin_bit_cast(v8bf, Ad[1]), B, z4);
        u64 v0 = (u64)pk2(d0[0], d0[1]) | ((u64)pk2(d0[2], d0[3]) << 32);
        u64 v1 = (u64)pk2(d1[0], d1[1]) | ((u64)pk2(d1[2], d1[3]) << 32);
        __hip_atomic_store((GAS u64*)(mb + o0 + f * 1024), v0, RLX_AGENT);
        if (g4 == 0) __hip_atomic_store((GAS u64*)(mb + o1 + f * 1024), v1, RLX_AGENT);
      }
      __builtin_amdgcn_s_setprio(0);
    } else if (wq == 3) {
      for (unsigned spins = 0; spins < (1u << 20); ++spins) {
        u64 v0 = __hip_atomic_load((GAS u64*)(fkv + lane * 8), RLX_AGENT);
        u64 v1 = __hip_atomic_load((GAS u64*)(fpan + r * 8), RLX_AGENT);
        const u64 b0 = __ballot(v0 == tag), b1 = __ballot(v1 == tag);
        unsigned st = (unsigned)b1 & 0xfff0u;
#pragma unroll
        for (int i = 0; i < 4; ++i) st |= (((b0 >> (16 * i)) & 0xffff) == 0xffff ? 1u : 0u) << i;
        asm volatile("" ::: "memory");
        if (lane == 0) *(volatile LAS unsigned*)(lb + P2_FLG + 16) = st;
        if (st == 0xffffu) break;
        __builtin_amdgcn_s_sleep(PSLEEP);
      }
    }
    wait_lds(lb + P2_FLG + 16, (1u << wq) | (16u << (wq >> 1)));
    if (wq != 0) ST(8 + 0);
    {
      const char* pq_ = ws + WS_PQ + ((size_t)(4 * mt2) * 20 + 5 * wq) * 1024;
      const char* zsrc = ws + WS_ZP + wq * 640;
      const unsigned zo = (lane < 40 ? lane : 0) * 16;
      f32x4 zs;
      {
        u32x4 zt[16];
#pragma unroll
        for (int i = 0; i < 16; ++i) gld16_sc1(zt[i], zo, zsrc + i * 2560);
#pragma unroll
        for (int s = 0; s < 5; ++s)
#pragma unroll
          for (int m = 0; m < 4; ++m) gld16_sc1(P[s][m], l16, pq_ + (size_t)(m * 20 + s) * 1024);
        ST(8 + 1);
        wait_vm4<20>(zt[0], zt[1], zt[2], zt[3]);
        pin4(zt[4], zt[5], zt[6], zt[7]); pin4(zt[8], zt[9], zt[10], zt[11]); pin4(zt[12], zt[13], zt[14], zt[15]);
        zs = __builtin_bit_cast(f32x4, zt[0]);
#pragma unroll
        for (int i = 1; i < 16; ++i) zs += __builtin_bit_cast(f32x4, zt[i]);
      }
      wait_vm4<0>(P[0][0], P[0][1], P[0][2], P[0][3]);
#pragma unroll
      for (int s = 1; s < 5; ++s) pin4(P[s][0], P[s][1], P[s][2], P[s][3]);
      for (unsigned spins = 0; spins < (1u << 16); ++spins) {
        const float t1 = (zs[0] + zs[1]) + (zs[2] + zs[3]);
        if (!__any(t1 != t1)) break;
        __builtin_amdgcn_s_sleep(4);
        u32x4 zt[16];
#pragma unroll
        for (int i = 0; i < 16; ++i) gld16_sc1(zt[i], zo, zsrc + i * 2560);
        wait_vm4<0>(zt[0], zt[1], zt[2], zt[3]);
        pin4(zt[4], zt[5], zt[6], zt[7]); pin4(zt[8], zt[9], zt[10], zt[11]); pin4(zt[12], zt[13], zt[14], zt[15]);
        zs = __builtin_bit_cast(f32x4, zt[0]);
#pragma unroll
        for (int i = 1; i < 16; ++i) zs += __builtin_bit_cast(f32x4, zt[i]);
      }
      if (lane < 40) *(LAS u32x2*)(zs_ + (lane >> 3) * 64 + (lane & 3) * 16 + ((lane >> 2) & 1) * 8) = (u32x2){pk2(zs[0], zs[1]), pk2(zs[2], zs[3])};
    }
    u32x4 Z[5];
#pragma unroll
    for (int s = 0; s < 5; ++s) Z[s] = *(LAS u32x4*)(zs_ + s * 64 + g4 * 16);
    f32x4 den[3][4];
#pragma unroll
    for (int T = 0; T < 3; ++T) {
      const int hneed = (4 * (4 * T + (r & 3)) + (r >> 2)) / 5;
#pragma unroll
      for (int m = 0; m < 4; ++m) den[T][m] = (f32x4){0.f, 0.f, 0.f, 0.f};
#pragma unroll
      for (int s = 0; s < 5; ++s) {
        if ((T == 0 && s > 2) || (T == 1 && s < 1) || (T == 2 && s < 3)) continue;
        bool k0 = ((8 * s + g4) / 5) == hneed, k1 = ((8 * s + 4 + g4) / 5) == hneed;
        u32x4 zf = {k0 ? Z[s][0] : 0u, k0 ? Z[s][1] : 0u, k1 ? Z[s][2] : 0u, k1 ? Z[s][3] : 0u};
#pragma unroll
        for (int m = 0; m < 4; ++m) den[T][m] = mfma16u(zf, P[s][m], den[T][m]);
      }
    }
#pragma unroll
    for (int s = 0; s < 5; ++s)
#pragma unroll
      for (int m = 0; m < 4; ++m) {
        float ra = __builtin_amdgcn_rcpf(den[(2 * s) >> 2][m][(2 * s) & 3] + 1e-6f);
        float rb = __builtin_amdgcn_rcpf(den[(2 * s + 1) >> 2][m][(2 * s + 1) & 3] + 1e-6f);
        u32x4 v = P[s][m];
        P[s][m] = (u32x4){pk2(blo(v[0]) * ra, bhi(v[0]) * ra), pk2(blo(v[1]) * ra, bhi(v[1]) * ra),
                          pk2(blo(v[2]) * rb, bhi(v[2]) * rb), pk2(blo(v[3]) * rb, bhi(v[3]) * rb)};
      }
#pragma unroll
    for (int s = 0; s < 5; ++s) pin4(P[s][0], P[s][1], P[s][2], P[s][3]);
    ST(8 + 2);
    const char* mp = ws + WS_M + ((size_t)c * 100 + 25 * wq) * 1024;
    for (unsigned spins = 0; spins < (1u << 16); ++spins) {
      u32x4 Mf[5][5];
#pragma unroll
      for (int s = 0; s < 3; ++s)
#pragma unroll
        for (int f = 0; f < 5; ++f) gld16_sc1(Mf[s][f], l16, mp + (s * 5 + f) * 1024);
      zero_acc();
      sfor<5>([&](auto SC) {
        constexpr int s = SC;
        constexpr int allowed = s <= 2 ? 10 : (s == 3 ? 5 : 0);
        SB;
        wait_vm4<allowed>(Mf[s][0], Mf[s][1], Mf[s][2], Mf[s][3]); pin1(Mf[s][4]);
#pragma unroll
        for (int f = 0; f < 5; ++f)
#pragma unroll
          for (int m = 0; m < 4; ++m) mfma_au(acc[m][f], Mf[s][f], P[s][m]);
        SB;
        if constexpr (s < 2) {
#pragma unroll
          for (int f = 0; f < 5; ++f) gld16_sc1(Mf[s + 3][f], l16, mp + (15 + 5 * s + f) * 1024);
        }
      });
      asm volatile("s_nop 15" ::: "memory");
      SB;
      const f32x4 t4 = (acc[0][0] + acc[0][1]) + (acc[0][2] + acc[0][3]) + acc[0][4];
      const float t1 = (t4[0] + t4[1]) + (t4[2] + t4[3]);
      if (!__any(t1 != t1)) break;
      ST(8 + 3);
      __builtin_amdgcn_s_sleep(4);
    }
    ST(8 + 4);
    if (wq != 0) {
      for (unsigned spins = 0; spins < (1u << 22); ++spins) {
        unsigned v = *(volatile LAS unsigned*)(lb + P2_FLG + (lane & 3) * 4);
        if (__all(v == 1u)) break;
        __builtin_amdgcn_s_sleep(1);
      }
      asm volatile("" ::: "memory");
    }
    const unsigned pa = wq == 0 ? lb + P2_SCR : lb + (16 + 4 * (wq - 1)) * SLOT;
#pragma unroll
    for (int m = 0; m < 4; ++m)
#pragma unroll
      for (int f = 0; f < 5; ++f)
        if (m * 5 + f != 16 || wq != 0) *(LAS f32x4*)(pa + (m * 5 + f) * 1024 + l16) = acc[m][f];
  }
  wait_lgkm0();
  bar();
  ST(8 + 5);
#pragma unroll
  for (int i = 0; i < 3; ++i) {
    const int t = w + 8 * i;
    if (t < 20) {
      const int m = t / 5, f = t - 5 * m;
      f32x4 s = *(LAS f32x4*)(lb + P2_SCR + t * 1024 + l16);
      if (i == 2 && w == 0) s = acc[3][1];
      f32x4 d = *(LAS f32x4*)(lb + (4 * m) * SLOT + f * 1024 + l16);
#pragma unroll
      for (int k = 1; k < 4; ++k) {
        s += *(LAS f32x4*)(lb + (16 + 4 * (k - 1)) * SLOT + t * 1024 + l16);
        d += *(LAS f32x4*)(lb + (4 * m + k) * SLOT + f * 1024 + l16);
      }
      float xo[4] = {blo(xres[i][0]), bhi(xres[i][0]), blo(xres[i][1]), bhi(xres[i][1])};
      float res[4];
#pragma unroll
      for (int jj = 0; jj < 4; ++jj) {
        float g = d[jj] * __builtin_amdgcn_rcpf(1.0f + __expf(-d[jj]));
        res[jj] = __builtin_fmaf(s[jj], g, xo[jj]);
      }
      *(u32x2*)(out + (size_t)(64 * mt2 + 16 * m + r) * 640 + 80 * c + 16 * f + 4 * g4) = (u32x2){pk2(res[0], res[1]), pk2(res[2], res[3])};
    }
  }
  ST(8 + 6);
  asm volatile(".Lfk_end:" ::: "memory");
}

extern "C" void kernel_launch(void* const* d_in, const int* in_sizes, int n_in, void* d_out, int out_size, void* d_ws,
                              size_t ws_size, hipStream_t stream) {
  fused_kernel<<<256, 512, 0, stream>>>((const bf16*)d_in[0], (const bf16*)d_in[1], (const bf16*)d_in[2], (const bf16*)d_in[4],
                                        (const bf16*)d_in[5], (const bf16*)d_in[6], (char*)d_ws, (bf16*)d_out);
}
```

```cpp
#define MAP81
#define WDW 19
#define NOLATEB
#include <hip/hip_runtime.h>
#include <stdint.h>
#include <utility>

typedef __bf16 bf16;
typedef __bf16 v8bf __attribute__((ext_vector_type(8)));
typedef __bf16 v4bf __attribute__((ext_vector_type(4)));
typedef __bf16 v2bf __attribute__((ext_vector_type(2)));
typedef float f32x4 __attribute__((ext_vector_type(4)));
typedef float f32x2 __attribute__((ext_vector_type(2)));
typedef unsigned u32x4 __attribute__((ext_vector_type(4)));
typedef unsigned u32x2 __attribute__((ext_vector_type(2)));
typedef unsigned long long u64;
#define LAS __attribute__((address_space(3)))
#define GAS __attribute__((address_space(1)))
#define DEVI __device__ __forceinline__
#define SB __builtin_amdgcn_sched_barrier(0)
#define IC(n) std::integral_constant<int, (n)>{}
#define RLX_AGENT __ATOMIC_RELAXED, __HIP_MEMORY_SCOPE_AGENT

template <class F, int... I> DEVI void sfor_impl(F&& f, std::integer_sequence<int, I...>) { (f(std::integral_constant<int, I>{}), ...); }
template <int N, class F> DEVI void sfor(F&& f) { sfor_impl(f, std::make_integer_sequence<int, N>{}); }

constexpr size_t WS_H   = 0;
constexpr size_t WS_PQ  = WS_H + 128ull * 48 * 1024;
constexpr size_t WS_KV  = WS_PQ + 128ull * 20 * 1024;
constexpr size_t WS_ZP  = WS_KV + 32ull * 16 * 1600;
constexpr size_t WS_M   = WS_ZP + 65536;
constexpr size_t WS_FP  = WS_M + 8ull * 100 * 1024;
constexpr size_t WS_FK  = WS_FP + 32768;
constexpr size_t WS_FE  = WS_FK + 4096;
constexpr size_t WS_EP  = WS_FE + 32768;
constexpr size_t WS_RS  = WS_EP + 1024;
constexpr size_t WS_CS  = WS_RS + 327680;
constexpr size_t WS_WD  = WS_CS + 327680;
constexpr size_t WS_DUM = WS_WD + 8ull * 240 * 1024;
constexpr int NPOISON = 1664;

DEVI v8bf cat8(v4bf a, v4bf b) { return __builtin_shufflevector(a, b, 0, 1, 2, 3, 4, 5, 6, 7); }
DEVI v4bf trd(unsigned addr) { return __builtin_amdgcn_ds_read_tr16_b64_v4bf16((LAS v4bf*)addr); }
DEVI v8bf ldsr16(unsigned addr) { return *(LAS v8bf*)addr; }
DEVI f32x4 mfma16(v8bf a, v8bf b, f32x4 c) { return __builtin_amdgcn_mfma_f32_16x16x32_bf16(a, b, c, 0, 0, 0); }
DEVI void mfma_a(f32x4& c, v8bf a, v8bf b) { asm volatile("v_mfma_f32_16x16x32_bf16 %0, %1, %2, %0" : "+v"(c) : "v"(a), "v"(b)); }
DEVI void mfma_au(f32x4& c, u32x4 a, u32x4 b) { asm volatile("v_mfma_f32_16x16x32_bf16 %0, %1, %2, %0" : "+v"(c) : "v"(a), "v"(b)); }
DEVI unsigned pk2(float lo, float hi) { v2bf v = {(bf16)lo, (bf16)hi}; return __builtin_bit_cast(unsigned, v); }
DEVI float blo(unsigned d) { return __uint_as_float(d << 16); }
DEVI float bhi(unsigned d) { return __uint_as_float(d & 0xffff0000u); }
DEVI f32x2 lo2(f32x4 v) { return (f32x2){v[0], v[1]}; }
DEVI f32x2 hi2(f32x4 v) { return (f32x2){v[2], v[3]}; }

DEVI void glds_full(unsigned ldsdst, unsigned voff, const void* sbase) {
  asm volatile("s_mov_b32 m0, %0\n\ts_nop 0\n\tglobal_load_lds_dwordx4 %1, %2" ::"s"(ldsdst), "v"(voff), "s"(sbase) : "memory");
}
DEVI void glds_q(unsigned ldsdst, unsigned voff, const void* sbase) {
  asm volatile("s_mov_b32 m0, %0\n\ts_mov_b64 exec, 0xffff\n\tglobal_load_lds_dwordx4 %1, %2\n\ts_mov_b64 exec, -1" ::"s"(ldsdst), "v"(voff), "s"(sbase) : "memory");
}
DEVI void glds_sc1(unsigned ldsdst, unsigned voff, const void* sbase) {
  asm volatile("s_mov_b32 m0, %0\n\ts_nop 0\n\tglobal_load_lds_dwordx4 %1, %2 sc1" ::"s"(ldsdst), "v"(voff), "s"(sbase) : "memory");
}
template <int N> DEVI void wait_vm() { asm volatile("s_waitcnt vmcnt(%0)" ::"n"(N) : "memory"); }
DEVI void wait_lgkm0() { asm volatile("s_waitcnt lgkmcnt(0)" ::: "memory"); }
DEVI void bar() { __builtin_amdgcn_s_barrier(); }
template <int OFF> DEVI void gld16_sc1(u32x4& d, unsigned voff, const char* sbase) {
  asm volatile("global_load_dwordx4 %0, %1, %2 offset:%3 sc1" : "=v"(d) : "v"(voff), "s"(sbase + ((OFF >> 13) << 13) + 4096), "n"((OFF & 8191) - 4096) : "memory");
}
template <int OFF> DEVI void gld16_sc1r(u32x4& d, unsigned voff, const char* sbase) {
  asm volatile("global_load_dwordx4 %0, %1, %2 offset:%3 sc1" : "+v"(d) : "v"(voff), "s"(sbase + ((OFF >> 13) << 13) + 4096), "n"((OFF & 8191) - 4096) : "memory");
}
DEVI void gld16(u32x4& d, unsigned voff, const char* sbase) { asm volatile("global_load_dwordx4 %0, %1, %2" : "=v"(d) : "v"(voff), "s"(sbase) : "memory"); }
DEVI void gld16_lo20(u32x4& d, unsigned voff, const char* sbase) {
  asm volatile("s_mov_b64 exec, 0xfffff\n\tglobal_load_dwordx4 %0, %1, %2 sc1\n\ts_mov_b64 exec, -1" : "+v"(d) : "v"(voff), "s"(sbase) : "memory");
}
DEVI void gld8_sc1(u32x2& d, unsigned voff, const char* sbase) { asm volatile("global_load_dwordx2 %0, %1, %2 sc1" : "=v"(d) : "v"(voff), "s"(sbase) : "memory"); }
DEVI void pinf2(float& a, float& b) { asm volatile("" : "+v"(a), "+v"(b)); }
DEVI void pin2(f32x2& a, f32x2& b) { asm volatile("" : "+v"(a), "+v"(b)); }
DEVI void pinu2(u32x2& a) { asm volatile("" : "+v"(a)); }
DEVI void pinf4(f32x4& a, f32x4& b) { asm volatile("" : "+v"(a), "+v"(b)); }
template <int N> DEVI void wait_vm4(u32x4& a, u32x4& b, u32x4& c, u32x4& d) {
  asm volatile("s_waitcnt vmcnt(%4)" : "+v"(a), "+v"(b), "+v"(c), "+v"(d) : "n"(N) : "memory");
}
DEVI void pin4(u32x4& a, u32x4& b, u32x4& c, u32x4& d) { asm volatile("" : "+v"(a), "+v"(b), "+v"(c), "+v"(d)); }
DEVI void pin1(u32x4& a) { asm volatile("" : "+v"(a)); }
DEVI void st16(unsigned voff, const char* sbase, u32x4 v) { asm volatile("global_store_dwordx4 %0, %1, %2 sc1\n\ts_nop 1" ::"v"(voff), "v"(v), "s"(sbase) : "memory"); }
DEVI void st8(unsigned voff, const char* sbase, u32x2 v) { asm volatile("global_store_dwordx2 %0, %1, %2 sc1" ::"v"(voff), "v"(v), "s"(sbase) : "memory"); }
DEVI void st16m(unsigned mlo, unsigned mhi, unsigned voff, const char* sbase, u32x4 v) {
  asm volatile("s_mov_b32 exec_lo, %3\n\ts_mov_b32 exec_hi, %4\n\ts_nop 2\n\tglobal_store_dwordx4 %0, %1, %2 sc1\n\ts_mov_b64 exec, -1\n\ts_nop 0" ::"v"(voff), "v"(v), "s"(sbase), "s"(mlo), "s"(mhi) : "memory");
}
DEVI void st8m(unsigned mlo, unsigned mhi, unsigned voff, const char* sbase, u32x2 v) {
  asm volatile("s_mov_b32 exec_lo, %3\n\ts_mov_b32 exec_hi, %4\n\ts_nop 2\n\tglobal_store_dwordx2 %0, %1, %2 sc1\n\ts_mov_b64 exec, -1" ::"v"(voff), "v"(v), "s"(sbase), "s"(mlo), "s"(mhi) : "memory");
}
DEVI unsigned ltmask(int a, int b) { int d = a - b; asm("" : "+s"(d)); return (unsigned)(d >> 31); }
DEVI void st4(unsigned voff, const char* sbase, float v) { asm volatile("global_store_dword %0, %1, %2 sc1" ::"v"(voff), "v"(v), "s"(sbase) : "memory"); }
DEVI float xsum4(float x) {
  auto a = __builtin_amdgcn_permlane32_swap(__float_as_uint(x), __float_as_uint(x), false, false);
  unsigned a0 = a[0], a1 = a[1];
  float y = __uint_as_float(a0) + __uint_as_float(a1);
  auto b = __builtin_amdgcn_permlane16_swap(__float_as_uint(y), __float_as_uint(y), false, false);
  unsigned b0 = b[0], b1 = b[1];
  return __uint_as_float(b0) + __uint_as_float(b1);
}
template <int N> DEVI float ror_add(float x) {
  float y; asm("s_nop 1\n\tv_add_f32_dpp %0, %1, %1 row_ror:%2 row_mask:0xf bank_mask:0xf" : "=v"(y) : "v"(x), "n"(N)); return y;
}
DEVI void acc_bf2(float& a, float& b, unsigned d) {
  float t0, t1;
  asm volatile("v_lshlrev_b32 %2, 16, %4\n\tv_and_b32 %3, 0xffff0000, %4\n\tv_add_f32 %0, %0, %2\n\tv_add_f32 %1, %1, %3" : "+v"(a), "+v"(b), "=&v"(t0), "=&v"(t1) : "v"(d));
}
DEVI void acc_sq2(float& s, float& q, unsigned d) {
  float t0, t1;
  asm volatile("v_lshlrev_b32 %2, 16, %4\n\tv_and_b32 %3, 0xffff0000, %4\n\tv_add_f32 %0, %0, %2\n\tv_fmac_f32 %1, %2, %2\n\tv_add_f32 %0, %0, %3\n\tv_fmac_f32 %1, %3, %3"
               : "+v"(s), "+v"(q), "=&v"(t0), "=&v"(t1) : "v"(d));
}
DEVI void add4(f32x4& a, f32x4 b) {
  float a0 = a[0], a1 = a[1], a2 = a[2], a3 = a[3], b0 = b[0], b1 = b[1], b2 = b[2], b3 = b[3];
  asm volatile("v_add_f32 %0, %0, %4\n\tv_add_f32 %1, %1, %5\n\tv_add_f32 %2, %2, %6\n\tv_add_f32 %3, %3, %7" : "+v"(a0), "+v"(a1), "+v"(a2), "+v"(a3) : "v"(b0), "v"(b1), "v"(b2), "v"(b3));
  a = (f32x4){a0, a1, a2, a3};
}
DEVI float rsum16(float x) { return ror_add<1>(ror_add<2>(ror_add<4>(ror_add<8>(x)))); }
#define ZSLEEP 4
#define PSLEEP 6
DEVI void wait_flags(const char* p, u64 tag) {
  for (unsigned spins = 0; spins < (1u << 20); ++spins) {
    u64 v = __hip_atomic_load((GAS u64*)p, RLX_AGENT);
    if (__all(v == tag)) break;
    __builtin_amdgcn_s_sleep(PSLEEP);
  }
  asm volatile("" ::: "memory");
}
DEVI void wait_lds(unsigned addr, unsigned mask) {
  for (unsigned spins = 0; spins < (1u << 24); ++spins) {
    unsigned v = __builtin_amdgcn_readfirstlane(*(volatile LAS unsigned*)addr);
    if ((v & mask) == mask) break;
    __builtin_amdgcn_s_sleep(1);
  }
  asm volatile("" ::: "memory");
}

#define ST(i)
#define STS(i)

DEVI float exp2_clamp(float t) { float e; asm("v_exp_f32_e64 %0, %1 clamp" : "=v"(e) : "v"(t)); return e; }
DEVI f32x4 phi4(f32x4 t2) {
  f32x4 a = t2 * 1.4426950408889634f;
  f32x4 e = {exp2_clamp(a[0]), exp2_clamp(a[1]), exp2_clamp(a[2]), exp2_clamp(a[3])};
  f32x4 m = {__builtin_fmaxf(t2[0], 1e-6f), __builtin_fmaxf(t2[1], 1e-6f), __builtin_fmaxf(t2[2], 1e-6f), __builtin_fmaxf(t2[3], 1e-6f)};
  return m + e;
}
DEVI u32x2 pk4(f32x4 v) { return (u32x2){pk2(v[0], v[1]), pk2(v[2], v[3])}; }
DEVI unsigned geglu_pk(f32x2 g, f32x2 u, float c1, float c3, float ia2) {
  f32x2 t = g * (g * g * c3 + c1);
  f32x2 e = {__builtin_amdgcn_exp2f(t[0]), __builtin_amdgcn_exp2f(t[1])};
  f32x2 d = e * ia2 + ia2;
  f32x2 rr = {__builtin_amdgcn_rcpf(d[0]), __builtin_amdgcn_rcpf(d[1])};
  f32x2 o = (g * u) * rr;
  return pk2(o[0], o[1]);
}
DEVI u32x2 geglu4(f32x4 g, f32x4 u, float c1, float c3, float ia2) {
  return (u32x2){geglu_pk(lo2(g), lo2(u), c1, c3, ia2), geglu_pk(hi2(g), hi2(u), c1, c3, ia2)};
}

constexpr int XOFF = 98304, XSLOT = 16384;
constexpr int ST_WDT = 147456;
constexpr int ST_CSP = 155648;
constexpr int ST_SINK = 160768;
constexpr int ST_ONE = 162816;
constexpr int KVP = 88;
constexpr int KVI = 73728;
constexpr int PRS = 143360;
constexpr int SLOT = 5120;
constexpr int P2_SCR = 28 * SLOT;
constexpr int P2_FLG = P2_SCR + 16 * 1024;
constexpr int LDS_BYTES = 163840;

constexpr int side_ops(int NS, int s) {
  return (s == 0 && NS == 5 ? 2 : 0) + (s == 4 ? (NS == 5 ? 3 : 1) : 0) + (s == 5 && NS == 5 ? 2 : 0) + (s == 13 ? 1 : 0) + (s == 15 ? 4 : 0);
}
constexpr int dma_allowed(int NS, int s) {
  int n = 0, need = 0;
  const int nw = s + 1, nx = 2 * ((s + 1) >> 1) + 1;
  auto X = [&](int e) { n += 1; if (e <= nx) need = n; };
  auto W = [&](int st) { n += NS == 5 ? 2 : 3; if (st <= nw) need = n; };
  X(0); X(1); W(0); W(1); X(2); X(3); W(2);
  if (s >= 0) { W(3); X(4); if (NS == 5) W(4); }
  for (int t = 0; t < s; ++t) { if (t + 5 <= 19) X(t + 5); if (t + NS <= 19) W(t + NS); n += side_ops(NS, t); }
  return n - need;
}

struct Ctx {
  const bf16 *x, *wqkv, *wo, *wg, *wu, *wdn; char* ws;
  unsigned lb, l16; int tid, lane, w, tw, p, r, g4, q, pp, bid, xcd, idx, mt, ct, mt2, c;
};

template <int ROLE> DEVI void phase1(const Ctx& C, u64& tag) {
  constexpr int NF = ROLE ? 6 : 4;
  constexpr int NSW = ROLE ? 4 : 5, WSLOT = ROLE ? 24576 : 18432;
  constexpr int S3 = ROLE ? 8 : 10, G = 15;
  char* ws = C.ws;
  const unsigned lb = C.lb, l16 = C.l16;
  const int tid = C.tid, lane = C.lane, w = C.w, tw = C.tw, p = C.p, r = C.r, g4 = C.g4, q = C.q, pp = C.pp;
  const int bid = C.bid, xcd = C.xcd, idx = C.idx, mt = C.mt, ct = C.ct, mt2 = C.mt2, c = C.c;

  u32x2 epv;
  asm volatile("global_load_dwordx2 %0, %1, %2 sc1" : "=v"(epv) : "v"(0u), "s"(ws + WS_EP) : "memory");

  unsigned woff[3]; const char* wptr; unsigned wdst;
  constexpr unsigned wstep = ROLE ? 32u * 3072 : 32u * 3840;
  if constexpr (ROLE) {
#pragma unroll
    for (int i = 0; i < 3; ++i) {
      int L = 64 * i + lane, row = L / 24, ch = L - row * 24;
      ch ^= (((row >> 1) & 1) | ((w & 1) << 1)) << 1;
      woff[i] = (unsigned)(row * 3072 + ch * 16);
    }
    wptr = (const char*)(tw ? C.wu : C.wg) + (size_t)(8 * p) * 3072 + ct * 384;
    wdst = lb + tw * 12288 + p * 3072;
  } else {
#pragma unroll
    for (int i = 0; i < 3; ++i) {
      int L = i == 0 ? 1024 + 64 * (w & 1) + lane : 128 * w + 64 * (i - 1) + lane;
      int sg = L / 12, ch = L % 12, rg = sg >> 5, row = sg & 31;
      ch ^= ((row >> 3) & 1) << 1;
      woff[i] = (unsigned)(row * 3840 + rg * 1280 + ch * 16);
    }
    wptr = (const char*)C.wqkv + ct * 160 - (ct & 1) * 32;
    wdst = lb + w * 2048 - 1024;
  }
  const unsigned xoff = (unsigned)((lane >> 3) * 1280 + (((lane & 7) ^ ((4 * w + (lane >> 4)) & 7)) * 16));
  const char* xptr = (const char*)C.x + (size_t)(128 * mt + 8 * w) * 1280;
  const unsigned xdst = lb + XOFF + w * 1024;
  const int rot = (5 * (xcd >> 2) + ((0x7520 >> (4 * (xcd & 3))) & 15) + (idx & 3) + ((idx & 3) >> 1 & (idx & 1)) + 2 * (ct & 1)) % 10;
  auto issue_w = [&](int st, int i) {
    int kk = st + 2 * rot; kk = kk >= 20 ? kk - 20 : kk;
    unsigned so = (unsigned)kk * wstep; asm("" : "+s"(so));
    const char* b = wptr + so;
    unsigned d = wdst + (st % NSW) * WSLOT + i * 1024;
    if (ROLE || i > 0) glds_full(d, woff[i], b);
    else if ((w >> 1) == (st & 3)) glds_full(lb + (st % NSW) * WSLOT + 16384 + (w & 1) * 1024, woff[0], b);
  };
  auto issue_x = [&](int e) {
    const int J = e >> 1, h = e & 1;
    int kk = J + rot; kk = kk >= 10 ? kk - 10 : kk;
    glds_full(xdst + (J % 3) * XSLOT + h * 8192, xoff, xptr + h * 64 * 1280 + kk * 128);
  };
  auto issue_W = [&](int st) { issue_w(st, 0); issue_w(st, 1); issue_w(st, 2); };
  issue_x(0); issue_x(1); issue_W(0); issue_W(1); issue_x(2); issue_x(3); issue_W(2);
  const int gw = (bid ^ 128) * 8 + w;
  const int f1 = gw + 1024 < 1920 ? gw + 1024 : gw;
  const char* dst_pz = ws + WS_KV + (size_t)gw * 1024;
  const char* dst_wd = ws + WS_WD + (size_t)gw * 1024;
  const char* src_wd0 = (const char*)C.wdn + (size_t)(32 * ((gw % 240) / 5)) * 1280 + (gw / 240) * 160 + (gw % 5) * 32;
  const char* src_wd1 = (const char*)C.wdn + (size_t)(32 * ((f1 % 240) / 5)) * 1280 + (f1 / 240) * 160 + (f1 % 5) * 32;
  const unsigned wdt1 = lb + (w < 6 ? 92160 + w * 1024 : ST_CSP + (w - 6) * 1024);
  const int u1 = rot == 9 ? 0 : rot + 1;
  const char* dst_cs = ws + WS_CS + (size_t)((ROLE * 8 + ct) * 10 + (tw ? u1 : rot)) * 2048 + p * 384;
  const char* dst_rs0 = ws + WS_RS + (mt * 10 + rot) * 2048 + (64 * tw + 16 * p) * 8;
  const char* dst_rs1 = ws + WS_RS + (mt * 10 + u1) * 2048 + (64 * tw + 16 * p) * 8;
  unsigned v_wd = (unsigned)((lane >> 1) * 1280 + (lane & 1) * 16), v_tr = (4 * g4 + q) * 32 + pp * 8, v_cs = r * 64 + g4 * 16;
  asm volatile("" : "+s"(dst_cs), "+v"(v_cs));
  if constexpr (!ROLE) asm volatile("" : "+s"(dst_pz), "+s"(dst_wd), "+s"(src_wd0), "+s"(src_wd1), "+s"(dst_rs0), "+s"(dst_rs1), "+v"(v_wd), "+v"(v_tr));
  unsigned v_wo = 0;
  if constexpr (!ROLE) { int L = 64 * (w < 7 ? w : 6) + lane; L = L < 400 ? L : 399; v_wo = (unsigned)((L / 20) * 1280 + (L % 20) * 16); asm volatile("" : "+v"(v_wo)); }
  f32x4 cs, sx, gr; v8bf Xs; v4bf wt[4]; u32x2 rso[2];
  u32x4 sel[NF];
#pragma unroll
  for (int f = 0; f < NF; ++f) { const unsigned o = r == f ? 0x3f803f80u : 0u; sel[f] = (u32x4){o, o, o, o}; pin1(sel[f]); }
  const bf16 one = (bf16)1.0f;
  v8bf ones = {one, one, one, one, one, one, one, one};
  cs = sx = gr = (f32x4){0.f, 0.f, 0.f, 0.f};
  asm volatile("" : "+v"(cs));
  if constexpr (!ROLE) asm volatile("" : "+v"(ones), "+v"(sx), "+v"(gr));

  constexpr int NB = ROLE ? 3 : 2;
  unsigned wa[NB][2];
  if constexpr (ROLE) {
    const int swz = ((q >> 1) & 1) | ((g4 & 1) << 1);
    const unsigned rowb = lb + (8 * g4 + q) * 384 + pp * 8;
    wa[0][0] = rowb + ((2 * p) ^ swz) * 32; wa[1][0] = rowb + ((2 * p + 1) ^ swz) * 32; wa[2][0] = rowb + ((8 + p) ^ swz) * 32;
  } else {
    const unsigned rowb = lb + (8 * g4 + q) * 192, hb = (ct & 1) * 32 + 40 * p, sx = (g4 & 1) << 5;
    wa[0][0] = rowb + ((hb + 8 * pp) ^ sx);
    wa[1][0] = rowb + (pp < 2 ? pp : 2) * 6144 + ((hb + 32) ^ sx);
  }
#pragma unroll
  for (int i = 0; i < NB; ++i) { wa[i][1] = wa[i][0] + 2 * WSLOT; asm volatile("" : "+v"(wa[i][1])); }
  unsigned xa[2];
  {
    unsigned t = lb + XOFF + (64 * tw + r) * 128;
    xa[0] = t + ((g4 ^ ((r >> 1) & 7)) * 16);
    xa[1] = t + (((4 + g4) ^ ((r >> 1) & 7)) * 16);
  }
  f32x4 acc[4][NF];
#pragma unroll
  for (int m = 0; m < 4; ++m)
#pragma unroll
    for (int f = 0; f < NF; ++f) { acc[m][f] = (f32x4){0.f, 0.f, 0.f, 0.f}; asm volatile("" : "+v"(acc[m][f])); }

  const char* b1 = ws + WS_WD + (size_t)((c * 48 + 12 * p + tw) * 5) * 1024;
  const char* b2 = ws + WS_WD + (size_t)((c * 48 + 12 * p + 2) * 5) * 1024;
  u32x2 wfl;
  const char* fep = ws + WS_FE + (size_t)((idx & 15) * 256 + w * 32) * 8;
  u32x4 gl[4]; f32x4 gs, csv[NF]; f32x2 rsv[4];
  const int gg = lane < 60 ? lane / 20 : 0, gi = lane < 60 ? lane - 20 * gg : 0;
  const int gx = gi < 12 ? 12 * w + gi : 96 + 8 * w + gi - 12;
  const unsigned go = (gi < 12 ? (unsigned)(WS_CS - WS_RS + (ROLE * 8 + ct) * 20480) + gx * 16 : (unsigned)(mt * 20480 + (gx - 96) * 16)) + gg * 2048;

  v4bf Wh[2][NF][2]; v8bf Xf[2][4];
  auto rdW = [&](auto SC, auto FC, auto TC) {
    constexpr int s = SC, f = FC, t = TC; constexpr int b = s & 1;
    constexpr int sl = s % NSW;
    constexpr unsigned so = (sl >= 2 ? sl - 2 : sl) * WSLOT;
    if constexpr (ROLE) Wh[b][f][t] = trd(wa[f >> 1][sl >= 2] + so + (f & 1) * 12288 + t * 1536);
    else Wh[b][f][t] = trd(wa[f == 3][sl >= 2] + so + (f < 3 ? f * 6144 : 0) + t * 768);
  };
  auto rdX = [&](auto SC, auto MC) {
    constexpr int s = SC, m = MC; constexpr int b = s & 1;
    Xf[b][m] = ldsr16(xa[s & 1] + ((s >> 1) % 3) * XSLOT + m * 2048);
  };
  constexpr int NRD = 2 * NF + 4;
  auto rdI = [&](auto SC, auto IC_) {
    constexpr int i = IC_;
    if constexpr (i < 2) rdW(SC, IC(0), IC(i));
    else if constexpr (i < 6) rdX(SC, IC(i - 2));
    else rdW(SC, IC((i - 4) >> 1), IC(i & 1));
  };
  asm volatile("s_waitcnt vmcnt(%1)" : "+v"(epv) : "n"(dma_allowed(NSW, -1)) : "memory");
  wait_lgkm0();
  bar();
  ST(1);
  tag = (((u64)__builtin_amdgcn_readfirstlane(epv[1]) << 32) | (u64)(unsigned)__builtin_amdgcn_readfirstlane(epv[0])) + 0x9E3779B97F4A7C15ull;
  sfor<NRD>([&](auto I) { rdI(IC(0), I); });
  if constexpr (!ROLE) Xs = ldsr16(xa[0] + p * 2048);
  issue_W(3); issue_x(4);
  if constexpr (NSW == 5) issue_W(4);

  sfor<20>([&](auto SC) {
    constexpr int s = SC; constexpr int b = s & 1;
    constexpr bool more = s < 19;
    if constexpr (more) {
      SB;
      if constexpr (s == G) {
        asm volatile("s_waitcnt vmcnt(%1)" : "+v"(wfl) : "n"(ROLE ? 4 : 3) : "memory");
        const u64 got = ((u64)wfl[1] << 32) | wfl[0];
        if (!__all(got == tag)) wait_flags(fep + (lane & 31) * 8, tag);
      } else if constexpr (s == 17) wait_vm4<0>(gl[0], gl[1], gl[2], gl[3]);
      else if constexpr (s < 17) wait_vm<dma_allowed(NSW, s)>();
      wait_lgkm0();
      bar();
      STS(s);
      if constexpr (s == S3)
        { if (w == 0) st8m(0xffffu, 0u, r * 2048, ws + WS_FE + bid * 8, (u32x2){(unsigned)tag, (unsigned)(tag >> 32)}); }
      if constexpr (s == 17) {
        if (gw == 0) st8m(1u, 0u, 0u, ws + WS_EP, (u32x2){(unsigned)tag, (unsigned)(tag >> 32)});
      }
    }
    SB;
    sfor<4 * NF>([&](auto KC) {
      constexpr int k = KC, f = k >> 2, m = k & 3;
      mfma_a(acc[m][f], cat8(Wh[b][f][0], Wh[b][f][1]), Xf[b][m]);
      SB;
      if constexpr (more && k < NRD) rdI(IC(s + 1), KC);
      if constexpr (s < 4 && k == 4 * NF - 1) {
        if (tw == (s >> 1)) sfor<NF>([&](auto F) { mfma_a(cs, cat8(Wh[b][F][0], Wh[b][F][1]), __builtin_bit_cast(v8bf, sel[F])); });
      }
      if constexpr (s == 4 && k == 11) st16m(ROLE ? 0x003f003fu : 0x000f000fu, ROLE ? 0x003f003fu : 0x000f000fu, v_cs, dst_cs, __builtin_bit_cast(u32x4, cs));
      if constexpr (!ROLE) {
        if constexpr ((s == 2 || s == 4) && k == 0) {
          const float g0 = gr[0], g1 = gr[1], g2 = gr[2], g3 = gr[3];
          const float d0 = (r & 1) ? g1 : g0, d1 = (r & 1) ? g3 : g2;
          rso[(s >> 1) - 1] = (u32x2){__float_as_uint(sx[0]), __float_as_uint((r & 2) ? d1 : d0)};
          pinu2(rso[(s >> 1) - 1]);
          if constexpr (s == 2) { sx = gr = (f32x4){0.f, 0.f, 0.f, 0.f}; pinf4(sx, gr); }
        }
        if constexpr (s < 4 && k == 2) { mfma_a(sx, ones, Xs); mfma_a(gr, Xs, Xs); }
        if constexpr (s < 3 && k == NRD) Xs = ldsr16(xa[(s + 1) & 1] + ((s + 1) >> 1) * XSLOT + p * 2048);
        if constexpr (s == 0 && k == 12) glds_full(lb + ST_WDT + w * 1024, v_wd, src_wd0);
        if constexpr (s == 0 && k == 14) glds_full(wdt1, v_wd, src_wd1);
        if constexpr (s == 4 && k == 13) st8m(0x00F0000Fu, 0xF0000F00u, r * 8, dst_rs0, rso[0]);
        if constexpr (s == 4 && k == 15) st8m(0x00F0000Fu, 0xF0000F00u, r * 8, dst_rs1, rso[1]);
        if constexpr (s == 5 && k == 2) { wt[0] = trd(lb + ST_WDT + w * 1024 + v_tr); wt[1] = trd(lb + ST_WDT + w * 1024 + 512 + v_tr); }
        if constexpr (s == 5 && k == 5) { wt[2] = trd(wdt1 + v_tr); wt[3] = trd(wdt1 + 512 + v_tr); }
        if constexpr (s == 5 && k == 11) st16(l16, dst_pz, (u32x4){~0u, ~0u, ~0u, ~0u});
        if constexpr (s == 5 && k == 12) st16(l16, dst_wd, __builtin_bit_cast(u32x4, cat8(wt[0], wt[1])));
        if constexpr (s == 5 && k == 13) { if (gw + 1024 < NPOISON) st16(l16, dst_pz + 1048576, (u32x4){~0u, ~0u, ~0u, ~0u}); }
        if constexpr (s == 5 && k == 15) { if (gw + 1024 < 1920) st16(l16, dst_wd + 1048576, __builtin_bit_cast(u32x4, cat8(wt[2], wt[3]))); }
      }
      if constexpr (s == G - 2 && k == 11) gld8_sc1(wfl, (unsigned)(lane & 31) * 8, fep);
      if constexpr (s == G && k >= 11 && k < 14) gld16_sc1<(k - 11) * 6144>(gl[k - 11], go, ws + WS_RS);
      if constexpr (s == G && k == 14) {
        gl[3] = (u32x4){0u, 0u, 0u, 0u};
        gld16_lo20(gl[3], go, ws + WS_RS + 18432);
      }
      if constexpr (!ROLE && s == 19 && k == 4)
        glds_full(lb + PRS + 1024 + (w < 7 ? w : 6) * 1024, v_wo, (const char*)C.wo + (size_t)(20 * (4 * ct + (mt & 3))) * 1280 + (mt >> 2) * 320);
      if constexpr (s == WDW && k == 5) {
        asm volatile("s_mov_b32 m0, %0\n\ts_nop 0\n\tglobal_load_lds_dword %1, %2 sc1" ::"s"(lb + ST_SINK + w * 256), "v"((unsigned)(((idx * 8 + w) * 60 + (lane < 60 ? lane : 59)) * 128)), "s"(ws + WS_WD) : "memory");
      }
      if constexpr (s == G + 2 && k < 3) { if constexpr (k == 0) gs = __builtin_bit_cast(f32x4, gl[0]); add4(gs, __builtin_bit_cast(f32x4, gl[k + 1])); }
      if constexpr (s == G + 2 && k == 3) *(LAS f32x4*)(lb + ST_WDT + (lane < 60 ? gg * 2560 + gx * 16 : 7680 + (lane - 60) * 16)) = gs;
      if constexpr (s == G + 3 && k == 3) {
        if (w < 3) {
          const unsigned o = (tid < 160 ? tid : 0) * 16;
          f32x4 t = *(LAS f32x4*)(lb + ST_WDT + o);
          add4(t, *(LAS f32x4*)(lb + ST_WDT + 2560 + o)); add4(t, *(LAS f32x4*)(lb + ST_WDT + 5120 + o));
          *(LAS f32x4*)(lb + ST_CSP + o) = t;
        }
      }
      if constexpr (s == 19 && k == 2) { wait_lgkm0(); bar(); }
      if constexpr (s == 19 && k >= 3 && k < 3 + NF) csv[k - 3] = *(LAS f32x4*)(lb + ST_CSP + (p * 6 + k - 3) * 64 + g4 * 16);
      if constexpr (s == 19 && k >= 3 + NF && k < 7 + NF) rsv[k - 3 - NF] = *(LAS f32x2*)(lb + ST_CSP + 1536 + (64 * tw + 16 * (k - 3 - NF) + r) * 8);
      if constexpr (k % 3 == 1 && k / 3 < 5) {
        constexpr int i = k / 3;
        if constexpr (s >= 17) {
          constexpr int n = (s - 17) * 5 + i;
          if constexpr (n < 5) glds_sc1(lb + w * SLOT + n * 1024, l16, b1 + n * 1024);
          else if constexpr (n < 8) {
            constexpr int i0 = n - 5, i1 = i0 == 0 ? 3 : 4;
            glds_sc1(lb + (8 + p) * SLOT + (tw ? i1 : i0) * 1024, l16, b2 + (tw ? i1 : i0) * 1024);
          }
        } else {
          if constexpr (i == 0 && s + 5 <= 19) issue_x(s + 5);
          if constexpr (i >= 1 && i <= 3 && s + NSW <= 19) issue_w(s + NSW, i - 1);
        }
      }
      SB;
    });
  });
  asm volatile("s_nop 15" ::: "memory");
  SB;
  ST(2);
  float ve[4], ra[4];
#pragma unroll
  for (int m = 0; m < 4; ++m) {
    const float mean = rsv[m][0] * (1.0f / 640.0f);
    ve[m] = __builtin_fmaxf(rsv[m][1] * (1.0f / 640.0f) - mean * mean, 0.f) + 1e-5f; ra[m] = __builtin_amdgcn_rsqf(ve[m]);
#pragma unroll
    for (int f = 0; f < NF; ++f) acc[m][f] -= csv[f] * mean;
  }

  if constexpr (ROLE) {
    ST(3);
    const float k1 = -2.0f * 0.7978845608028654f * 1.4426950408889634f, k3 = k1 * 0.044715f;
    const char* hb = ws + WS_H + (size_t)((8 * mt + 4 * tw) * 48 + 6 * ct) * 1024;
#pragma unroll
    for (int m = 0; m < 4; ++m) {
      const float c1 = ra[m] * k1, c3 = ra[m] * ra[m] * ra[m] * k3;
      u32x2 a = geglu4(acc[m][0], acc[m][1], c1, c3, ve[m]), b = geglu4(acc[m][2], acc[m][3], c1, c3, ve[m]);
      st16(l16, hb + (m * 48 + p) * 1024, (u32x4){a[0], a[1], b[0], b[1]});
      st8(l16, hb + (m * 48 + 4 + (p >> 1)) * 1024 + (p & 1) * 8, geglu4(acc[m][4], acc[m][5], c1, c3, ve[m]));
    }
    ST(4);
    wait_vm<0>();
    bar();
    if (tid < 16) __hip_atomic_store((GAS u64*)(ws + WS_FP + (size_t)((mt * 16 + tid) * 16 + 8 + ct) * 8), tag, RLX_AGENT);
    ST(5);
  } else {
    const int hd = 4 * ct + p;
    f32x4 x3[4];
#pragma unroll
    for (int m = 0; m < 4; ++m) {
      const unsigned ro = lb + KVI + p * (128 * KVP) + (64 * tw + 16 * m + r) * KVP;
      *(LAS u32x2*)(ro + 8 * g4) = pk4(phi4(acc[m][1] * ra[m] + 1e-6f));
      *(LAS u32x2*)(ro + 40 + 8 * g4) = pk4(acc[m][2] * ra[m]);
      const f32x4 t = acc[m][3] * ra[m];
      x3[m] = phi4(t + 1e-6f);
      const u32x2 e0 = pk4(x3[m]), e1 = pk4(t);
      if (g4 == 1 || g4 == 2) *(LAS u32x2*)(ro + (g4 == 1 ? 32 : 72)) = g4 == 1 ? e0 : e1;
    }
    wait_vm<2>();
    wait_lgkm0();
    bar();
    ST(3);
    {
      f32x4 t0 = {0.f, 0.f, 0.f, 0.f}, t1 = t0;
      const unsigned tb = lb + KVI + p * (128 * KVP) + (4 * g4 + q) * KVP;
      const unsigned kLo = tb + pp * 8, kHi = tb + 32;
      const bool one = tw && pp;
      const unsigned vA = one ? lb + ST_ONE : tb + 40 + (tw ? 32 : pp * 8), vs = one ? 0u : (unsigned)KVP;
#pragma unroll
      for (int ts = 0; ts < 4; ++ts) {
        v8bf kl = cat8(trd(kLo + ts * 32 * KVP), trd(kLo + (ts * 32 + 16) * KVP));
        v8bf kh = cat8(trd(kHi + ts * 32 * KVP), trd(kHi + (ts * 32 + 16) * KVP));
        v8bf vv = cat8(trd(vA + ts * 32 * vs), trd(vA + (ts * 32 + 16) * vs));
        t0 = mfma16(vv, kl, t0); t1 = mfma16(vv, kh, t1);
      }
      asm volatile("s_nop 7\n\ts_nop 7" : "+v"(t0), "+v"(t1));
      const char* kvp = ws + WS_KV + (size_t)(hd * 16 + mt) * 1600;
      const char* zp = ws + WS_ZP + (size_t)(hd * 16 + mt) * 128;
      if (tw == 0 || g4 == 0) {
        const unsigned eo = tw ? 64u : 16u * g4;
        st16(r * 80 + eo, kvp, __builtin_bit_cast(u32x4, t0));
        if (r < 4) st16((16 + r) * 80 + eo, kvp, __builtin_bit_cast(u32x4, t1));
      } else if (g4 == 1) {
        st4(r * 4, zp, t0[0]);
        if (r < 4) st4(64 + r * 4, zp, t1[0]);
      }
    }
    wait_vm<3>();
    bar();
    ST(4);
    if (w >= 4) {
      const char* b3 = ws + WS_WD + (size_t)((c * 48 + 12 * p + 3) * 5) * 1024;
#pragma unroll
      for (int i = 0; i < 20; ++i) glds_sc1(lb + (12 + 4 * (i / 5) + p) * SLOT + (i % 5) * 1024, l16, b3 + i * 1024);
    }
    if (w < 2) __builtin_amdgcn_s_setprio(3);
    f32x4 pq[4];
#pragma unroll
    for (int m = 0; m < 4; ++m) { pq[m] = phi4(acc[m][0] * ra[m] + 1e-6f); asm volatile("" : "+v"(pq[m])); }
    const int h = 4 * ct + (mt & 3);
    f32x4 z0, z1;
    {
      const char* zsrc = ws + WS_ZP + (size_t)hd * 2048;
      for (unsigned spins = 0; spins < (1u << 18); ++spins) {
        u32x4 a, b;
        gld16_sc1<0>(a, r * 128 + g4 * 16, zsrc); gld16_sc1<64>(b, r * 128, zsrc);
        asm volatile("s_waitcnt vmcnt(0)" : "+v"(a), "+v"(b) :: "memory");
        z0 = __builtin_bit_cast(f32x4, a); z1 = __builtin_bit_cast(f32x4, b);
        const f32x4 t4 = z0 + z1; const float t1 = (t4[0] + t4[1]) + (t4[2] + t4[3]);
        if (!__any(t1 != t1)) break;
        __builtin_amdgcn_s_sleep(ZSLEEP);
      }
#pragma unroll
      for (int j = 0; j < 4; ++j) { z0[j] = rsum16(z0[j]); z1[j] = rsum16(z1[j]); }
    }
    ST(6);
    u32x4 ka[16], kb[16];
    const char* kp = ws + WS_KV + (size_t)h * 16 * 1600;
    const unsigned l2 = (lane < 36 ? 64 + lane : 64) * 16;
    if (w < 2) sfor<16>([&](auto I) { gld16_sc1<I * 1600>(ka[I], l16, kp); gld16_sc1<I * 1600>(kb[I], l2, kp); });
    if (g4 != 0) z1 = (f32x4){0.f, 0.f, 0.f, 0.f};
    {
      u32x2 e[4], e3[4];
#pragma unroll
      for (int m = 0; m < 4; ++m) {
        const f32x4 t = pq[m] * z0 + x3[m] * z1;
        const float rd = __builtin_amdgcn_rcpf(xsum4((t[0] + t[1]) + (t[2] + t[3])) + 1e-6f);
        e[m] = pk4(pq[m] * rd); e3[m] = pk4(x3[m] * rd);
      }
      const int hl = hd & 7;
      const char* qb = ws + WS_PQ + (size_t)((8 * mt + 4 * tw) * 20 + 5 * (hd >> 3)) * 1024;
#pragma unroll
      for (int m = 0; m < 4; m += 2) {
        auto s0 = __builtin_amdgcn_permlane32_swap(e[m][0], e[m + 1][0], false, false);
        auto s1 = __builtin_amdgcn_permlane32_swap(e[m][1], e[m + 1][1], false, false);
        unsigned a0 = s0[0], b0 = s0[1], a1 = s1[0], b1 = s1[1];
        st16((unsigned)((m + (g4 >> 1)) * 20 * 1024 + (r + 16 * (2 * (hl & 1) + (g4 & 1))) * 16), qb + (hl >> 1) * 1024, (u32x4){a0, a1, b0, b1});
      }
      if (g4 == 0) {
#pragma unroll
        for (int m = 0; m < 4; ++m) st8((unsigned)((r + 16 * (hl & 3)) * 16 + (hl >> 2) * 8), qb + (m * 20 + 4) * 1024, e3[m]);
      }
    }
    if (w < 2) {
      const int hl = h & 7;
      const unsigned my = lb + PRS + w * 8192, wl = lb + PRS + 1024 + w * 160;
      f32x4 sa, sb;
      for (unsigned spins = 0; spins < (1u << 18); ++spins) {
        wait_vm4<0>(ka[0], ka[1], ka[2], ka[3]);
        pin4(ka[4], ka[5], ka[6], ka[7]); pin4(ka[8], ka[9], ka[10], ka[11]); pin4(ka[12], ka[13], ka[14], ka[15]);
        pin4(kb[0], kb[1], kb[2], kb[3]); pin4(kb[4], kb[5], kb[6], kb[7]); pin4(kb[8], kb[9], kb[10], kb[11]); pin4(kb[12], kb[13], kb[14], kb[15]);
        sa = __builtin_bit_cast(f32x4, ka[0]); sb = __builtin_bit_cast(f32x4, kb[0]);
#pragma unroll
        for (int i = 1; i < 16; ++i) { sa += __builtin_bit_cast(f32x4, ka[i]); sb += __builtin_bit_cast(f32x4, kb[i]); }
        const f32x4 t4 = sa + sb; const float t1 = (t4[0] + t4[1]) + (t4[2] + t4[3]);
        if (__builtin_expect(!__any(t1 != t1), 1)) break;
        sfor<16>([&](auto I) { gld16_sc1r<I * 1600>(ka[I], l16, kp); gld16_sc1r<I * 1600>(kb[I], l2, kp); });
        wait_vm<0>();
      }
      *(LAS u32x2*)(my + lane * 8) = pk4(sa);
      *(LAS u32x2*)(my + 512 + lane * 8) = pk4(sb);
      u32x4 Ad[2];
#pragma unroll
      for (int mf = 0; mf < 2; ++mf) {
        int d = mf == 0 ? r : 16 + (r & 3);
        u32x2 lo = *(LAS u32x2*)(my + d * 40 + g4 * 8);
        u32x2 hi = *(LAS u32x2*)(my + d * 40 + 32);
        Ad[mf] = (u32x4){lo[0], lo[1], g4 == 0 ? hi[0] : 0u, g4 == 0 ? hi[1] : 0u};
      }
      const char* mb = ws + WS_M + (size_t)((2 * (mt >> 2) + w) * 100 + 25 * (h >> 3)) * 1024;
      const unsigned o0 = (hl >> 1) * 5120 + (r + 16 * (2 * (hl & 1) + (g4 & 1))) * 16 + (g4 >> 1) * 8;
      const unsigned o1 = 4 * 5120 + (r + 16 * (hl & 3)) * 16 + (hl >> 2) * 8;
#pragma unroll
      for (int f = 0; f < 5; ++f) {
        v8bf B = cat8(trd(wl + (4 * g4 + q) * 320 + pp * 8 + f * 32), trd(wl + (16 + q) * 320 + pp * 8 + f * 32));
        f32x4 z4 = {0.f, 0.f, 0.f, 0.f};
        f32x4 d0 = mfma16(__builtin_bit_cast(v8bf, Ad[0]), B, z4), d1 = mfma16(__builtin_bit_cast(v8bf, Ad[1]), B, z4);
        const char* dst = mb + f * 1024;
        st8(o0, dst, pk4(d0));
        if (g4 == 0) st8(o1, dst, pk4(d1));
      }
      __builtin_amdgcn_s_setprio(0);
    } else wait_vm<0>();
    ST(7);
    {
      unsigned old = 0;
      if (lane == 0) old = __hip_atomic_fetch_add((LAS unsigned*)(lb + P2_FLG + 20), 1u, __ATOMIC_RELAXED, __HIP_MEMORY_SCOPE_WORKGROUP);
      if (__builtin_amdgcn_readfirstlane(old) == 7 && lane < 16)
        __hip_atomic_store((GAS u64*)(ws + WS_FP + (size_t)((mt * 16 + lane) * 16 + ct) * 8), tag, RLX_AGENT);
    }
    ST(5);
  }
}

__global__ __launch_bounds__(512, 2) void fused_kernel(const bf16* __restrict__ x, const bf16* __restrict__ wqkv,
                                                       const bf16* __restrict__ wo, const bf16* __restrict__ wg,
                                                       const bf16* __restrict__ wu, const bf16* __restrict__ wdn,
                                                       char* __restrict__ ws, bf16* __restrict__ out) {
  __shared__ __attribute__((aligned(16))) char smem[LDS_BYTES];
  Ctx C;
  C.x = x; C.wqkv = wqkv; C.wo = wo; C.wg = wg; C.wu = wu; C.wdn = wdn; C.ws = ws;
  const unsigned lb = (unsigned)(uintptr_t)(LAS char*)smem;
  const int tid = threadIdx.x, lane = tid & 63;
  const int w = __builtin_amdgcn_readfirstlane(tid >> 6);
  const int r = lane & 15, g4 = lane >> 4;
  const int bid = blockIdx.x, xcd = bid & 7, idx = bid >> 3;
  const unsigned l16 = lane * 16;
  const int role = (idx >> 4) ^ 1;
  const int mt2 = 4 * xcd + (idx >> 3), c = idx & 7;
  C.lb = lb; C.l16 = l16; C.tid = tid; C.lane = lane; C.w = w; C.tw = w >> 2; C.p = w & 3; C.r = r; C.g4 = g4;
  C.q = (lane & 15) >> 2; C.pp = lane & 3; C.bid = bid; C.xcd = xcd; C.idx = idx;
  C.mt = 8 * (xcd >> 2) + (idx & 7); C.ct = 2 * (xcd & 3) + ((idx >> 3) & 1); C.mt2 = mt2; C.c = c;
  ST(0);
  {
    u64 pc; unsigned len;
    asm volatile("s_getpc_b64 %0\n.Lfk_begin:\n\ts_mov_b32 %1, .Lfk_end-.Lfk_begin" : "=s"(pc), "=s"(len));
    unsigned off = (unsigned)((idx * 8 + w) * 3 + (lane < 3 ? lane : 0)) * 128; off = off < len ? off : 0;
    asm volatile("s_mov_b32 m0, %0\n\ts_nop 0\n\tglobal_load_lds_dword %1, %2" ::"s"(lb + ST_SINK + w * 256), "v"(off), "s"(pc) : "memory");
  }
  if (tid < 8) *(LAS unsigned*)(lb + P2_FLG + tid * 4) = 0u;
  if (tid < 16) *(LAS unsigned*)(lb + ST_ONE + tid * 4) = 0x3f803f80u;

  u64 tag;
  if (role) phase1<1>(C, tag); else phase1<0>(C, tag);

  f32x4 acc[4][5];
  auto zero_acc = [&]() {
#pragma unroll
    for (int m = 0; m < 4; ++m) {
#pragma unroll
      for (int f = 0; f < 5; ++f) acc[m][f] = (f32x4){0.f, 0.f, 0.f, 0.f};
      asm volatile("" : "+v"(acc[m][0]), "+v"(acc[m][1]), "+v"(acc[m][2]), "+v"(acc[m][3]), "+v"(acc[m][4]));
    }
    asm volatile("s_nop 1" ::: "memory");
  };
  u32x2 xres[3];
  auto load_x = [&]() {
#pragma unroll
    for (int i = 0; i < 3; ++i) {
      int t = w + 8 * i; t = t < 20 ? t : w;
      const int m = t / 5, f = t - 5 * m;
      xres[i] = *(const u32x2*)(x + (size_t)(64 * mt2 + 16 * m + r) * 640 + 80 * c + 16 * f + 4 * g4);
    }
    asm volatile("" ::: "memory");
  };

  if (w >= 4) {
    const int j = w - 4;
    const char* bp = ws + WS_WD + (size_t)((c * 48 + 12 * j) * 5) * 1024;
    const char* ap = ws + WS_H + ((size_t)(4 * mt2) * 48 + 12 * j) * 1024;
    u32x4 A[3][4], Br[3][5], Bc[2][5];
    auto issue_a = [&](auto SC) {
      constexpr int s = SC;
      sfor<4>([&](auto M) { gld16_sc1<(M * 48 + s) * 1024>(A[s % 3][M], l16, ap); });
    };
    unsigned rb[3];
    rb[0] = lb + j * SLOT + l16; rb[1] = rb[0] + 12 * SLOT; rb[2] = rb[0] + 24 * SLOT;
    asm volatile("" : "+v"(rb[1]), "+v"(rb[2]));
    auto rdB = [&](u32x4 (&B)[5], int pos) {
#pragma unroll
      for (int f = 0; f < 5; ++f) B[f] = *(LAS u32x4*)(rb[pos / 3] + 4 * (pos % 3) * SLOT + f * 1024);
    };
    if (role) {
#pragma unroll
      for (int i = 0; i < 20; ++i) glds_sc1(lb + (12 + 4 * (i / 5) + j) * SLOT + (i % 5) * 1024, l16, bp + (15 + i) * 1024);
    }
    sfor<15>([&](auto I) { gld16_sc1<(35 + I) * 1024>(Br[I / 5][I % 5], l16, bp); });
    ST(8 + 2);
    wait_lds(lb + P2_FLG + 16, 0x300u << (2 * j));
    ST(8 + 1);
    sfor<3>([&](auto SC) { issue_a(SC); });
    zero_acc();
    wait_vm4<12>(Br[0][0], Br[0][1], Br[0][2], Br[0][3]);
    pin4(Br[0][4], Br[1][0], Br[1][1], Br[1][2]); pin4(Br[1][3], Br[1][4], Br[2][0], Br[2][1]); pin1(Br[2][2]); pin1(Br[2][3]); pin1(Br[2][4]);
    rdB(Bc[0], 0);
    sfor<12>([&](auto SC) {
      constexpr int s = SC; constexpr int b = s % 3;
      constexpr auto gs = [](int g) { return g <= 11 ? (g == 9 ? 14 : 4) : 0; };
      SB;
      wait_vm4<gs(s + 1) + gs(s + 2)>(A[b][0], A[b][1], A[b][2], A[b][3]);
      if constexpr (s >= 10) { pin4(Bc[s & 1][0], Bc[s & 1][1], Bc[s & 1][2], Bc[s & 1][3]); pin1(Bc[s & 1][4]); }
      if constexpr (s + 1 <= 6) rdB(Bc[(s + 1) & 1], s + 1);
      SB;
#pragma unroll
      for (int f = 0; f < 5; ++f)
#pragma unroll
        for (int m = 0; m < 4; ++m) mfma_au(acc[m][f], (s >= 7 && s <= 9) ? Br[(s >= 7 && s <= 9) ? s - 7 : 0][f] : Bc[s & 1][f], A[b][m]);
      SB;
      if constexpr (s == 6) {
        if (lane == 0) *(volatile LAS unsigned*)(lb + P2_FLG + j * 4) = 1u;
        sfor<10>([&](auto I) { gld16_sc1<(50 + I) * 1024>(Bc[I / 5][I % 5], l16, bp); });
      }
      if constexpr (s + 3 < 12) issue_a(IC(s + 3));
      if constexpr (s == 9) load_x();
    });
    asm volatile("s_nop 15" ::: "memory");
    SB;
    ST(8 + 4);
#pragma unroll
    for (int m = 0; m < 4; ++m)
#pragma unroll
      for (int f = 0; f < 5; ++f) *(LAS f32x4*)(lb + (4 * m + j) * SLOT + f * 1024 + l16) = acc[m][f];
  } else {
    const int wq = w;
    if (wq == 3) {
      const char* fpan = ws + WS_FP + (size_t)(((mt2 >> 1) * 16 + (mt2 & 1) * 8 + c) * 16) * 8;
      for (unsigned spins = 0; spins < (1u << 20); ++spins) {
        u64 v1 = __hip_atomic_load((GAS u64*)(fpan + r * 8), RLX_AGENT);
        const unsigned st = (unsigned)__ballot(v1 == tag) & 0xffffu;
        asm volatile("" ::: "memory");
        if (lane == 0) *(volatile LAS unsigned*)(lb + P2_FLG + 16) = st;
        if (st == 0xffffu) break;
        __builtin_amdgcn_s_sleep(PSLEEP);
      }
    }
    load_x();
    wait_lds(lb + P2_FLG + 16, 3u << (2 * wq));
    ST(8 + 0);
    const char* pq_ = ws + WS_PQ + ((size_t)(4 * mt2) * 20 + 5 * wq) * 1024;
    const char* mp = ws + WS_M + ((size_t)c * 100 + 25 * wq) * 1024;
    for (unsigned spins = 0; spins < (1u << 16); ++spins) {
      u32x4 P[3][4], Mf[3][5];
      auto issue = [&](auto SC) {
        constexpr int s = SC;
        sfor<5>([&](auto F) { gld16_sc1<(5 * s + F) * 1024>(Mf[s % 3][F], l16, mp); });
        sfor<4>([&](auto M) { gld16_sc1<(M * 20 + s) * 1024>(P[s % 3][M], l16, pq_); });
      };
      sfor<3>([&](auto SC) { issue(SC); });
      zero_acc();
      sfor<5>([&](auto SC) {
        constexpr int s = SC, b = s % 3;
        SB;
        wait_vm4<(s <= 2 ? 18 : (s == 3 ? 9 : 0))>(Mf[b][0], Mf[b][1], Mf[b][2], Mf[b][3]); pin1(Mf[b][4]);
        pin4(P[b][0], P[b][1], P[b][2], P[b][3]);
#pragma unroll
        for (int f = 0; f < 5; ++f)
#pragma unroll
          for (int m = 0; m < 4; ++m) mfma_au(acc[m][f], Mf[b][f], P[b][m]);
        SB;
        if constexpr (s < 2) issue(IC(s + 3));
      });
      asm volatile("s_nop 15" ::: "memory");
      SB;
      const f32x4 t4 = (acc[0][0] + acc[0][1]) + (acc[0][2] + acc[0][3]) + acc[0][4];
      const float t1 = (t4[0] + t4[1]) + (t4[2] + t4[3]);
      if (__builtin_expect(!__any(t1 != t1), 1)) break;
      ST(8 + 3);
      __builtin_amdgcn_s_sleep(8);
    }
    ST(8 + 4);
    if (wq != 0) {
      for (unsigned spins = 0; spins < (1u << 24); ++spins) {
        unsigned v = *(volatile LAS unsigned*)(lb + P2_FLG + (lane & 3) * 4);
        if (__all(v == 1u)) break;
        __builtin_amdgcn_s_sleep(1);
      }
      asm volatile("" ::: "memory");
    }
    if (wq == 0 && !role) wait_lds(lb + P2_FLG + 20, 8u);
    const unsigned pa = wq == 0 ? lb + P2_SCR : lb + (16 + 4 * (wq - 1)) * SLOT;
#pragma unroll
    for (int m = 0; m < 4; ++m)
#pragma unroll
      for (int f = 0; f < 5; ++f)
        if (m * 5 + f != 16 || wq != 0) *(LAS f32x4*)(pa + (m * 5 + f) * 1024 + l16) = acc[m][f];
  }
  wait_lgkm0();
  bar();
  ST(8 + 5);
#pragma unroll
  for (int i = 0; i < 3; ++i) {
    const int t = w + 8 * i;
    if (t < 20) {
      const int m = t / 5, f = t - 5 * m;
      f32x4 s = *(LAS f32x4*)(lb + P2_SCR + t * 1024 + l16);
      if (i == 2 && w == 0) s = acc[3][1];
      f32x4 d = *(LAS f32x4*)(lb + (4 * m) * SLOT + f * 1024 + l16);
#pragma unroll
      for (int k = 1; k < 4; ++k) {
        s += *(LAS f32x4*)(lb + (16 + 4 * (k - 1)) * SLOT + t * 1024 + l16);
        d += *(LAS f32x4*)(lb + (4 * m + k) * SLOT + f * 1024 + l16);
      }
      float xo[4] = {blo(xres[i][0]), bhi(xres[i][0]), blo(xres[i][1]), bhi(xres[i][1])};
      float res[4];
#pragma unroll
      for (int jj = 0; jj < 4; ++jj) {
        float g = d[jj] * __builtin_amdgcn_rcpf(1.0f + __expf(-d[jj]));
        res[jj] = __builtin_fmaf(s[jj], g, xo[jj]);
      }
      *(u32x2*)(out + (size_t)(64 * mt2 + 16 * m + r) * 640 + 80 * c + 16 * f + 4 * g4) = (u32x2){pk2(res[0], res[1]), pk2(res[2], res[3])};
    }
  }
  ST(8 + 6);
  asm volatile(".Lfk_end:" ::: "memory");
}

extern "C" void kernel_launch(void* const* d_in, const int* in_sizes, int n_in, void* d_out, int out_size, void* d_ws,
                              size_t ws_size, hipStream_t stream) {
  fused_kernel<<<256, 512, 0, stream>>>((const bf16*)d_in[0], (const bf16*)d_in[1], (const bf16*)d_in[2], (const bf16*)d_in[4],
                                        (const bf16*)d_in[5], (const bf16*)d_in[6], (char*)d_ws, (bf16*)d_out);
}
```

```cpp
#include <hip/hip_runtime.h>
#include <stdint.h>
#include <utility>

typedef __bf16 bf16;
typedef __bf16 v8bf __attribute__((ext_vector_type(8)));
typedef __bf16 v4bf __attribute__((ext_vector_type(4)));
typedef __bf16 v2bf __attribute__((ext_vector_type(2)));
typedef float f32x4 __attribute__((ext_vector_type(4)));
typedef float f32x2 __attribute__((ext_vector_type(2)));
typedef unsigned u32x4 __attribute__((ext_vector_type(4)));
typedef unsigned u32x2 __attribute__((ext_vector_type(2)));
typedef unsigned long long u64;
#define LAS __attribute__((address_space(3)))
#define GAS __attribute__((address_space(1)))
#define DEVI __device__ __forceinline__
#define SB __builtin_amdgcn_sched_barrier(0)
#define IC(n) std::integral_constant<int, (n)>{}
#define RLX_AGENT __ATOMIC_RELAXED, __HIP_MEMORY_SCOPE_AGENT

template <class F, int... I> DEVI void sfor_impl(F&& f, std::integer_sequence<int, I...>) { (f(std::integral_constant<int, I>{}), ...); }
template <int N, class F> DEVI void sfor(F&& f) { sfor_impl(f, std::make_integer_sequence<int, N>{}); }

constexpr size_t WS_H   = 0;
constexpr size_t WS_PQ  = WS_H + 128ull * 48 * 1024;
constexpr size_t WS_KV  = WS_PQ + 128ull * 20 * 1024;
constexpr size_t WS_ZP  = WS_KV + 32ull * 16 * 1600;
constexpr size_t WS_M   = WS_ZP + 65536;
constexpr size_t WS_FP  = WS_M + 8ull * 100 * 1024;
constexpr size_t WS_FE  = WS_FP + 32768;
constexpr size_t WS_EP  = WS_FE + 32768;
constexpr size_t WS_RS  = WS_EP + 1024;
constexpr size_t WS_CS  = WS_RS + 327680;
constexpr size_t WS_WD  = WS_CS + 327680;
constexpr int NPOISON = 1664;

DEVI v8bf cat8(v4bf a, v4bf b) { return __builtin_shufflevector(a, b, 0, 1, 2, 3, 4, 5, 6, 7); }
DEVI v4bf trd(unsigned addr) { return __builtin_amdgcn_ds_read_tr16_b64_v4bf16((LAS v4bf*)addr); }
DEVI v8bf ldsr16(unsigned addr) { return *(LAS v8bf*)addr; }
DEVI f32x4 mfma16(v8bf a, v8bf b, f32x4 c) { return __builtin_amdgcn_mfma_f32_16x16x32_bf16(a, b, c, 0, 0, 0); }
DEVI void mfma_a(f32x4& c, v8bf a, v8bf b) { asm volatile("v_mfma_f32_16x16x32_bf16 %0, %1, %2, %0" : "+v"(c) : "v"(a), "v"(b)); }
DEVI void mfma_au(f32x4& c, u32x4 a, u32x4 b) { asm volatile("v_mfma_f32_16x16x32_bf16 %0, %1, %2, %0" : "+v"(c) : "v"(a), "v"(b)); }
DEVI unsigned pk2(float lo, float hi) { v2bf v = {(bf16)lo, (bf16)hi}; return __builtin_bit_cast(unsigned, v); }
DEVI float blo(unsigned d) { return __uint_as_float(d << 16); }
DEVI float bhi(unsigned d) { return __uint_as_float(d & 0xffff0000u); }
DEVI f32x2 lo2(f32x4 v) { return (f32x2){v[0], v[1]}; }
DEVI f32x2 hi2(f32x4 v) { return (f32x2){v[2], v[3]}; }

DEVI void glds_full(unsigned ldsdst, unsigned voff, const void* sbase) {
  asm volatile("s_mov_b32 m0, %0\n\ts_nop 0\n\tglobal_load_lds_dwordx4 %1, %2" ::"s"(ldsdst), "v"(voff), "s"(sbase) : "memory");
}
DEVI void glds_sc1(unsigned ldsdst, unsigned voff, const void* sbase) {
  asm volatile("s_mov_b32 m0, %0\n\ts_nop 0\n\tglobal_load_lds_dwordx4 %1, %2 sc1" ::"s"(ldsdst), "v"(voff), "s"(sbase) : "memory");
}
template <int N> DEVI void wait_vm() { asm volatile("s_waitcnt vmcnt(%0)" ::"n"(N) : "memory"); }
DEVI void wait_lgkm0() { asm volatile("s_waitcnt lgkmcnt(0)" ::: "memory"); }
DEVI void bar() { __builtin_amdgcn_s_barrier(); }
template <int OFF> DEVI void gld16_sc1(u32x4& d, unsigned voff, const char* sbase) {
  asm volatile("global_load_dwordx4 %0, %1, %2 offset:%3 sc1" : "=v"(d) : "v"(voff), "s"(sbase + ((OFF >> 13) << 13) + 4096), "n"((OFF & 8191) - 4096) : "memory");
}
template <int OFF> DEVI void gld16_sc1r(u32x4& d, unsigned voff, const char* sbase) {
  asm volatile("global_load_dwordx4 %0, %1, %2 offset:%3 sc1" : "+v"(d) : "v"(voff), "s"(sbase + ((OFF >> 13) << 13) + 4096), "n"((OFF & 8191) - 4096) : "memory");
}
DEVI void touch(unsigned ldsdst, unsigned voff, const char* sbase) {
  asm volatile("s_mov_b32 m0, %0\n\ts_nop 0\n\tglobal_load_lds_dword %1, %2 sc1" ::"s"(ldsdst), "v"(voff), "s"(sbase) : "memory");
}
DEVI void gld16_lo20(u32x4& d, unsigned voff, const char* sbase) {
  asm volatile("s_mov_b64 exec, 0xfffff\n\tglobal_load_dwordx4 %0, %1, %2 sc1\n\ts_mov_b64 exec, -1" : "+v"(d) : "v"(voff), "s"(sbase) : "memory");
}
DEVI void gld8x2_sc1(u32x4& d, unsigned voff, const char* sbase) {
  u32x2 a, b;
  asm volatile("global_load_dwordx2 %0, %2, %3 sc1\n\tglobal_load_dwordx2 %1, %2, %3 offset:512 sc1" : "=&v"(a), "=&v"(b) : "v"(voff), "s"(sbase) : "memory");
  d = (u32x4){a[0], a[1], b[0], b[1]};
}
DEVI void gld8_sc1(u32x2& d, unsigned voff, const char* sbase) { asm volatile("global_load_dwordx2 %0, %1, %2 sc1" : "=v"(d) : "v"(voff), "s"(sbase) : "memory"); }
DEVI void pinu2(u32x2& a) { asm volatile("" : "+v"(a)); }
DEVI void pinf4(f32x4& a, f32x4& b) { asm volatile("" : "+v"(a), "+v"(b)); }
template <int N> DEVI void wait_vm4(u32x4& a, u32x4& b, u32x4& c, u32x4& d) {
  asm volatile("s_waitcnt vmcnt(%4)" : "+v"(a), "+v"(b), "+v"(c), "+v"(d) : "n"(N) : "memory");
}
DEVI void pin4(u32x4& a, u32x4& b, u32x4& c, u32x4& d) { asm volatile("" : "+v"(a), "+v"(b), "+v"(c), "+v"(d)); }
DEVI void pin1(u32x4& a) { asm volatile("" : "+v"(a)); }
DEVI void st16(unsigned voff, const char* sbase, u32x4 v) { asm volatile("global_store_dwordx4 %0, %1, %2 sc1\n\ts_nop 1" ::"v"(voff), "v"(v), "s"(sbase) : "memory"); }
DEVI void st8(unsigned voff, const char* sbase, u32x2 v) { asm volatile("global_store_dwordx2 %0, %1, %2 sc1" ::"v"(voff), "v"(v), "s"(sbase) : "memory"); }
DEVI void st16m(unsigned mlo, unsigned mhi, unsigned voff, const char* sbase, u32x4 v) {
  asm volatile("s_mov_b32 exec_lo, %3\n\ts_mov_b32 exec_hi, %4\n\ts_nop 2\n\tglobal_store_dwordx4 %0, %1, %2 sc1\n\ts_mov_b64 exec, -1\n\ts_nop 0" ::"v"(voff), "v"(v), "s"(sbase), "s"(mlo), "s"(mhi) : "memory");
}
DEVI void st8m(unsigned mlo, unsigned mhi, unsigned voff, const char* sbase, u32x2 v) {
  asm volatile("s_mov_b32 exec_lo, %3\n\ts_mov_b32 exec_hi, %4\n\ts_nop 2\n\tglobal_store_dwordx2 %0, %1, %2 sc1\n\ts_mov_b64 exec, -1" ::"v"(voff), "v"(v), "s"(sbase), "s"(mlo), "s"(mhi) : "memory");
}
DEVI void st4(unsigned voff, const char* sbase, float v) { asm volatile("global_store_dword %0, %1, %2 sc1" ::"v"(voff), "v"(v), "s"(sbase) : "memory"); }
DEVI float xsum4(float x) {
  auto a = __builtin_amdgcn_permlane32_swap(__float_as_uint(x), __float_as_uint(x), false, false);
  unsigned a0 = a[0], a1 = a[1];
  float y = __uint_as_float(a0) + __uint_as_float(a1);
  auto b = __builtin_amdgcn_permlane16_swap(__float_as_uint(y), __float_as_uint(y), false, false);
  unsigned b0 = b[0], b1 = b[1];
  return __uint_as_float(b0) + __uint_as_float(b1);
}
template <int N> DEVI float ror_add(float x) {
  float y; asm("s_nop 1\n\tv_add_f32_dpp %0, %1, %1 row_ror:%2 row_mask:0xf bank_mask:0xf" : "=v"(y) : "v"(x), "n"(N)); return y;
}
DEVI void add4(f32x4& a, f32x4 b) {
  float a0 = a[0], a1 = a[1], a2 = a[2], a3 = a[3], b0 = b[0], b1 = b[1], b2 = b[2], b3 = b[3];
  asm volatile("v_add_f32 %0, %0, %4\n\tv_add_f32 %1, %1, %5\n\tv_add_f32 %2, %2, %6\n\tv_add_f32 %3, %3, %7" : "+v"(a0), "+v"(a1), "+v"(a2), "+v"(a3) : "v"(b0), "v"(b1), "v"(b2), "v"(b3));
  a = (f32x4){a0, a1, a2, a3};
}
DEVI float rsum16(float x) { return ror_add<1>(ror_add<2>(ror_add<4>(ror_add<8>(x)))); }
constexpr int ZFIRST = 4, ZSLEEP = 4, PSLEEP = 6;
DEVI void wait_flags(const char* p, u64 tag) {
  for (unsigned spins = 0; spins < (1u << 20); ++spins) {
    u64 v = __hip_atomic_load((GAS u64*)p, RLX_AGENT);
    if (__all(v == tag)) break;
    __builtin_amdgcn_s_sleep(PSLEEP);
  }
  asm volatile("" ::: "memory");
}
DEVI void wait_lds(unsigned addr, unsigned mask) {
  for (unsigned spins = 0; spins < (1u << 24); ++spins) {
    unsigned v = __builtin_amdgcn_readfirstlane(*(volatile LAS unsigned*)addr);
    if ((v & mask) == mask) break;
    __builtin_amdgcn_s_sleep(1);
  }
  asm volatile("" ::: "memory");
}

DEVI float exp2_clamp(float t) { float e; asm("v_exp_f32_e64 %0, %1 clamp" : "=v"(e) : "v"(t)); return e; }
DEVI f32x4 phi4(f32x4 t2) {
  f32x4 a = t2 * 1.4426950408889634f;
  f32x4 e = {exp2_clamp(a[0]), exp2_clamp(a[1]), exp2_clamp(a[2]), exp2_clamp(a[3])};
  f32x4 m = {__builtin_fmaxf(t2[0], 1e-6f), __builtin_fmaxf(t2[1], 1e-6f), __builtin_fmaxf(t2[2], 1e-6f), __builtin_fmaxf(t2[3], 1e-6f)};
  return m + e;
}
DEVI u32x2 pk4(f32x4 v) { return (u32x2){pk2(v[0], v[1]), pk2(v[2], v[3])}; }
DEVI unsigned geglu_pk(f32x2 g, f32x2 u, float c1, float c3, float ia2) {
  f32x2 t = g * (g * g * c3 + c1);
  f32x2 e = {__builtin_amdgcn_exp2f(t[0]), __builtin_amdgcn_exp2f(t[1])};
  f32x2 d = e * ia2 + ia2;
  f32x2 rr = {__builtin_amdgcn_rcpf(d[0]), __builtin_amdgcn_rcpf(d[1])};
  f32x2 o = (g * u) * rr;
  return pk2(o[0], o[1]);
}
DEVI u32x2 geglu4(f32x4 g, f32x4 u, float c1, float c3, float ia2) {
  return (u32x2){geglu_pk(lo2(g), lo2(u), c1, c3, ia2), geglu_pk(hi2(g), hi2(u), c1, c3, ia2)};
}

constexpr int XOFF = 98304, XSLOT = 16384;
constexpr int ST_WDT = 147456;
constexpr int ST_CSP = 155648;
constexpr int ST_SINK = 160768;
constexpr int ST_ONE = 162816;
constexpr int KVP = 88;
constexpr int KVI = 97280;
constexpr int PRS = 143360;
constexpr int SLOT = 5120;
constexpr int P2_SCR = 28 * SLOT;
constexpr int P2_FLG = P2_SCR + 16 * 1024;
constexpr int LDS_BYTES = 163840;

constexpr int side_ops(int NS, int s) {
  return (s == 0 ? 1 : 0) + (s == 4 ? (NS == 5 ? 1 : 4) : 0) + (s == 13 ? 1 : 0) + (s == 15 ? 4 : 0);
}
constexpr int dma_allowed(int NS, int s) {
  int n = 0, need = 0;
  const int nw = s + 1, nx = 2 * ((s + 1) >> 1) + 1;
  auto X = [&](int e) { n += 1; if (e <= nx) need = n; };
  auto W = [&](int st) { n += NS == 5 ? 2 : 3; if (st <= nw) need = n; };
  X(0); X(1); W(0); W(1); X(2); X(3); W(2);
  if (s >= 0) { W(3); X(4); if (NS == 5) W(4); }
  for (int t = 0; t < s; ++t) { if (t + 5 <= 19) X(t + 5); if (t + NS <= 19) W(t + NS); n += side_ops(NS, t); }
  return n - need;
}

struct Ctx {
  const bf16 *x, *wqkv, *wo, *wg, *wu, *wdn; char* ws;
  unsigned lb, l16; int tid, lane, w, tw, p, r, g4, q, pp, bid, xcd, idx, mt, ct, mt2, c;
};

template <int ROLE> DEVI void phase1(const Ctx& C, u64& tag) {
  constexpr int NF = ROLE ? 6 : 4;
  constexpr int NSW = ROLE ? 4 : 5, WSLOT = ROLE ? 24576 : 18432;
  constexpr int S3 = ROLE ? 7 : 9, G = 15;
  char* ws = C.ws;
  const unsigned lb = C.lb, l16 = C.l16;
  const int tid = C.tid, lane = C.lane, w = C.w, tw = C.tw, p = C.p, r = C.r, g4 = C.g4, q = C.q, pp = C.pp;
  const int bid = C.bid, xcd = C.xcd, idx = C.idx, mt = C.mt, ct = C.ct, mt2 = C.mt2, c = C.c;

  u32x2 epv;
  asm volatile("global_load_dwordx2 %0, %1, %2 sc1" : "=v"(epv) : "v"(0u), "s"(ws + WS_EP) : "memory");

  unsigned woff[3]; const char* wptr; unsigned wdst;
  constexpr unsigned wstep = ROLE ? 32u * 3072 : 32u * 3840;
  if constexpr (ROLE) {
#pragma unroll
    for (int i = 0; i < 3; ++i) {
      int L = 64 * i + lane, row = L / 24, ch = L - row * 24;
      ch ^= (((row >> 1) & 1) | ((w & 1) << 1)) << 1;
      woff[i] = (unsigned)(row * 3072 + ch * 16);
    }
    wptr = (const char*)(tw ? C.wu : C.wg) + (size_t)(8 * p) * 3072 + ct * 384;
    wdst = lb + tw * 12288 + p * 3072;
  } else {
#pragma unroll
    for (int i = 0; i < 3; ++i) {
      int L = i == 0 ? 1024 + 64 * (w & 1) + lane : 128 * w + 64 * (i - 1) + lane;
      int sg = L / 12, ch = L % 12, rg = sg >> 5, row = sg & 31;
      ch ^= ((row >> 3) & 1) << 1;
      woff[i] = (unsigned)(row * 3840 + rg * 1280 + ch * 16);
    }
    wptr = (const char*)C.wqkv + ct * 160 - (ct & 1) * 32;
    wdst = lb + w * 2048 - 1024;
  }
  const unsigned xoff = (unsigned)((lane >> 3) * 1280 + (((lane & 7) ^ ((4 * w + (lane >> 4)) & 7)) * 16));
  const char* xptr = (const char*)C.x + (size_t)(128 * mt + 8 * w) * 1280;
  const unsigned xdst = lb + XOFF + w * 1024;
  const int rot = (5 * (xcd >> 2) + ((0x7520 >> (4 * (xcd & 3))) & 15) + (idx & 3) + ((idx & 3) >> 1 & (idx & 1)) + 2 * (ct & 1)) % 10;
  auto issue_w = [&](int st, int i) {
    int kk = st + 2 * rot; kk = kk >= 20 ? kk - 20 : kk;
    unsigned so = (unsigned)kk * wstep; asm("" : "+s"(so));
    const char* b = wptr + so;
    unsigned d = wdst + (st % NSW) * WSLOT + i * 1024;
    if (ROLE || i > 0) glds_full(d, woff[i], b);
    else if ((w >> 1) == (st & 3)) glds_full(lb + (st % NSW) * WSLOT + 16384 + (w & 1) * 1024, woff[0], b);
  };
  auto issue_x = [&](int e) {
    const int J = e >> 1, h = e & 1;
    int kk = J + rot; kk = kk >= 10 ? kk - 10 : kk;
    glds_full(xdst + (J % 3) * XSLOT + h * 8192, xoff, xptr + h * 64 * 1280 + kk * 128);
  };
  auto issue_W = [&](int st) { issue_w(st, 0); issue_w(st, 1); issue_w(st, 2); };
  issue_x(0); issue_x(1); issue_W(0); issue_W(1); issue_x(2); issue_x(3); issue_W(2);
  const int gw = (bid ^ 128) * 8 + w;
  const int f0 = gw < 1920 ? gw : 0;
  const char* dst_pz = ws + WS_KV + (size_t)(gw & 1023) * 1024;
  const char* dst_wd = ws + WS_WD + (size_t)gw * 1024;
  const char* src_wd0 = (const char*)C.wdn + (size_t)(32 * ((f0 % 240) / 5)) * 1280 + (f0 / 240) * 160 + (f0 % 5) * 32;
  const unsigned wsp = ((f0 % 240) / 5) % 6 >= 4 ? 512u : 256u;
  const int u1 = rot == 9 ? 0 : rot + 1;
  const char* dst_cs = ws + WS_CS + (size_t)((ROLE * 8 + ct) * 10 + (tw ? u1 : rot)) * 2048 + p * 384;
  const char* dst_rs0 = ws + WS_RS + (mt * 10 + rot) * 2048 + (64 * tw + 16 * p) * 8;
  const char* dst_rs1 = ws + WS_RS + (mt * 10 + u1) * 2048 + (64 * tw + 16 * p) * 8;
  unsigned v_cs = r * 64 + g4 * 16;
  asm volatile("" : "+s"(dst_cs), "+v"(v_cs), "+s"(dst_wd), "+s"(src_wd0));
  unsigned v_wo = 0;
  if constexpr (!ROLE) { int L = 64 * (w < 7 ? w : 6) + lane; L = L < 400 ? L : 399; v_wo = (unsigned)((L / 20) * 1280 + (L % 20) * 16); asm volatile("" : "+v"(v_wo)); }
  f32x4 cs, sx, gr; v8bf Xs; v4bf wt[4]; u32x2 rso[2];
  u32x4 sel[NF];
#pragma unroll
  for (int f = 0; f < NF; ++f) { const unsigned o = r == f ? 0x3f803f80u : 0u; sel[f] = (u32x4){o, o, o, o}; pin1(sel[f]); }
  const bf16 one = (bf16)1.0f;
  v8bf ones = {one, one, one, one, one, one, one, one};
  cs = sx = gr = (f32x4){0.f, 0.f, 0.f, 0.f};
  asm volatile("" : "+v"(cs));
  if constexpr (ROLE) asm volatile("" : "+v"(ones), "+v"(sx), "+v"(gr));

  constexpr int NB = ROLE ? 3 : 2;
  unsigned wa[NB][2];
  if constexpr (ROLE) {
    const int swz = ((q >> 1) & 1) | ((g4 & 1) << 1);
    const unsigned rowb = lb + (8 * g4 + q) * 384 + pp * 8;
    wa[0][0] = rowb + ((2 * p) ^ swz) * 32; wa[1][0] = rowb + ((2 * p + 1) ^ swz) * 32; wa[2][0] = rowb + ((8 + p) ^ swz) * 32;
  } else {
    const unsigned rowb = lb + (8 * g4 + q) * 192, hb = (ct & 1) * 32 + 40 * p, sx = (g4 & 1) << 5;
    wa[0][0] = rowb + ((hb + 8 * pp) ^ sx);
    wa[1][0] = rowb + (pp < 2 ? pp : 2) * 6144 + ((hb + 32) ^ sx);
  }
#pragma unroll
  for (int i = 0; i < NB; ++i) { wa[i][1] = wa[i][0] + 2 * WSLOT; asm volatile("" : "+v"(wa[i][1])); }
  unsigned xa[2];
  {
    unsigned t = lb + XOFF + (64 * tw + r) * 128;
    xa[0] = t + ((g4 ^ ((r >> 1) & 7)) * 16);
    xa[1] = t + (((4 + g4) ^ ((r >> 1) & 7)) * 16);
  }
  f32x4 acc[4][NF];
#pragma unroll
  for (int m = 0; m < 4; ++m)
#pragma unroll
    for (int f = 0; f < NF; ++f) { acc[m][f] = (f32x4){0.f, 0.f, 0.f, 0.f}; asm volatile("" : "+v"(acc[m][f])); }

  const char* b1 = ws + WS_WD + (size_t)((c * 48 + 12 * p + tw) * 5) * 1024;
  const char* b2 = ws + WS_WD + (size_t)((c * 48 + 12 * p + 2) * 5) * 1024;
  u32x2 wfl;
  const char* fep = ws + WS_FE + (size_t)((idx & 15) * 256 + w * 32) * 8;
  u32x4 gl[4]; f32x4 gs, csv[NF]; f32x2 rsv[4];
  const int gg = lane < 60 ? lane / 20 : 0, gi = lane < 60 ? lane - 20 * gg : 0;
  const int gx = gi < 12 ? 12 * w + gi : 96 + 8 * w + gi - 12;
  const unsigned go = (gi < 12 ? (unsigned)(WS_CS - WS_RS + (ROLE * 8 + ct) * 20480) + gx * 16 : (unsigned)(mt * 20480 + (gx - 96) * 16)) + gg * 2048;

  v4bf Wh[2][NF][2]; v8bf Xf[2][4];
  auto rdW = [&](auto SC, auto FC, auto TC) {
    constexpr int s = SC, f = FC, t = TC; constexpr int b = s & 1;
    constexpr int sl = s % NSW;
    constexpr unsigned so = (sl >= 2 ? sl - 2 : sl) * WSLOT;
    if constexpr (ROLE) Wh[b][f][t] = trd(wa[f >> 1][sl >= 2] + so + (f & 1) * 12288 + t * 1536);
    else Wh[b][f][t] = trd(wa[f == 3][sl >= 2] + so + (f < 3 ? f * 6144 : 0) + t * 768);
  };
  auto rdX = [&](auto SC, auto MC) {
    constexpr int s = SC, m = MC; constexpr int b = s & 1;
    Xf[b][m] = ldsr16(xa[s & 1] + ((s >> 1) % 3) * XSLOT + m * 2048);
  };
  constexpr int NRD = 2 * NF + 4;
  auto rdI = [&](auto SC, auto IC_) {
    constexpr int i = IC_;
    if constexpr (i < 2) rdW(SC, IC(0), IC(i));
    else if constexpr (i < 6) rdX(SC, IC(i - 2));
    else rdW(SC, IC((i - 4) >> 1), IC(i & 1));
  };
  asm volatile("s_waitcnt vmcnt(%1)" : "+v"(epv) : "n"(dma_allowed(NSW, -1)) : "memory");
  wait_lgkm0();
  bar();
  tag = (((u64)__builtin_amdgcn_readfirstlane(epv[1]) << 32) | (u64)(unsigned)__builtin_amdgcn_readfirstlane(epv[0])) + 0x9E3779B97F4A7C15ull;
  sfor<NRD>([&](auto I) { rdI(IC(0), I); });
  if constexpr (ROLE) Xs = ldsr16(xa[0] + p * 2048);
  issue_W(3); issue_x(4);
  if constexpr (NSW == 5) issue_W(4);

  sfor<20>([&](auto SC) {
    constexpr int s = SC; constexpr int b = s & 1;
    constexpr bool more = s < 19;
    if constexpr (more) {
      SB;
      if constexpr (s == G) {
        asm volatile("s_waitcnt vmcnt(%1)" : "+v"(wfl) : "n"(ROLE ? 4 : 3) : "memory");
        const u64 got = ((u64)wfl[1] << 32) | wfl[0];
        if (!__all(got == tag)) wait_flags(fep + (lane & 31) * 8, tag);
      } else if constexpr (s == 17) wait_vm4<(ROLE ? 0 : 2)>(gl[0], gl[1], gl[2], gl[3]);
      else if constexpr (s == S3) wait_vm<0>();
      else if constexpr (s < 17) wait_vm<dma_allowed(NSW, s)>();
      wait_lgkm0();
      bar();
      if constexpr (s == S3)
        { if (w == 0) st8m(0xffffu, 0u, r * 2048, ws + WS_FE + bid * 8, (u32x2){(unsigned)tag, (unsigned)(tag >> 32)}); }
      if constexpr (s == 17) {
        if (gw == 0) st8m(1u, 0u, 0u, ws + WS_EP, (u32x2){(unsigned)tag, (unsigned)(tag >> 32)});
      }
    }
    SB;
    sfor<4 * NF>([&](auto KC) {
      constexpr int k = KC, f = k >> 2, m = k & 3;
      mfma_a(acc[m][f], cat8(Wh[b][f][0], Wh[b][f][1]), Xf[b][m]);
      SB;
      if constexpr (more && k < NRD) rdI(IC(s + 1), KC);
      if constexpr (s < 4 && k == 4 * NF - 1) {
        if (tw == (s >> 1)) sfor<NF>([&](auto F) { mfma_a(cs, cat8(Wh[b][F][0], Wh[b][F][1]), __builtin_bit_cast(v8bf, sel[F])); });
      }
      if constexpr (s == 4 && k == 11) st16m(ROLE ? 0x003f003fu : 0x000f000fu, ROLE ? 0x003f003fu : 0x000f000fu, v_cs, dst_cs, __builtin_bit_cast(u32x4, cs));
      if constexpr (s == 0 && k == 12) glds_full(lb + ST_WDT + w * 1024, (unsigned)((lane >> 1) * 1280 + (lane & 1) * 16), src_wd0);
      if constexpr (s == NSW && k == 3) {
        unsigned ln = lane; asm volatile("" : "+v"(ln));
        const unsigned a0 = lb + ST_WDT + w * 1024 + (ln >> 5) * wsp + (ln & 31) * 8;
        wt[0] = trd(a0); wt[1] = trd(a0 + 768 - wsp);
      }
      if constexpr (s == NSW && k == 4 * NF - 2) { if (gw < 1920) st16(l16, dst_wd, __builtin_bit_cast(u32x4, cat8(wt[0], wt[1]))); }
      if constexpr (ROLE) {
        if constexpr ((s == 2 || s == 4) && k == 0) {
          const float g0 = gr[0], g1 = gr[1], g2 = gr[2], g3 = gr[3];
          const float d0 = (r & 1) ? g1 : g0, d1 = (r & 1) ? g3 : g2;
          rso[(s >> 1) - 1] = (u32x2){__float_as_uint(sx[0]), __float_as_uint((r & 2) ? d1 : d0)};
          pinu2(rso[(s >> 1) - 1]);
          if constexpr (s == 2) { sx = gr = (f32x4){0.f, 0.f, 0.f, 0.f}; pinf4(sx, gr); }
        }
        if constexpr (s < 4 && k == 2) { mfma_a(sx, ones, Xs); mfma_a(gr, Xs, Xs); }
        if constexpr (s < 3 && k == NRD) Xs = ldsr16(xa[(s + 1) & 1] + ((s + 1) >> 1) * XSLOT + p * 2048);
        if constexpr (s == 4 && k == 13) st8m(0x00F0000Fu, 0xF0000F00u, r * 8, dst_rs0, rso[0]);
        if constexpr (s == 4 && k == 15) st8m(0x00F0000Fu, 0xF0000F00u, r * 8, dst_rs1, rso[1]);
        if constexpr (s == 4 && k == 17) st16(l16, dst_pz, (u32x4){~0u, ~0u, ~0u, ~0u});
        if constexpr (s == 4 && k == 19) { if (gw < NPOISON) st16(l16, dst_pz + 1048576, (u32x4){~0u, ~0u, ~0u, ~0u}); }
      }
      if constexpr (s == 1 && k == 12) { if (w == 3) touch(lb + ST_SINK, 0u, ws + WS_FE + bid * 128); }
      if constexpr (s == 11 && k == 12) {
        if (w == 3) touch(lb + ST_SINK, 0u, ws + WS_FP + (size_t)((mt2 >> 1) * 16 + (mt2 & 1) * 8 + c) * 128);
      }
      if constexpr (s == G - 2 && k == 11) gld8_sc1(wfl, (unsigned)(lane & 31) * 8, fep);
      if constexpr (s == G && k >= 11 && k < 14) gld16_sc1<(k - 11) * 6144>(gl[k - 11], go, ws + WS_RS);
      if constexpr (s == G && k == 14) {
        gl[3] = (u32x4){0u, 0u, 0u, 0u};
        gld16_lo20(gl[3], go, ws + WS_RS + 18432);
      }
      if constexpr (ROLE && s == 19 && k >= 4 && k < 24 && !(k & 1)) {
        constexpr int i = (k - 4) / 2;
        glds_sc1(lb + (12 + 8 * tw + 4 * (i / 5) + p) * SLOT + (i % 5) * 1024, l16, b2 + (5 + 10 * tw + i) * 1024);
      }
      if constexpr (!ROLE && s == 19 && k == 13)
        glds_full(lb + PRS + 1024 + (w < 7 ? w : 6) * 1024, v_wo, (const char*)C.wo + (size_t)(20 * (4 * ct + (mt & 3))) * 1280 + (mt >> 2) * 320);
      if constexpr (!ROLE && s == 16 && (k == 2 || k == 8))
        touch(lb + ST_SINK + w * 256, (unsigned)((((idx & 15) * 8 + w) * 120 + (k == 8 ? 60 : 0) + (lane < 60 ? lane : 59)) * 128), ws + WS_WD);
      if constexpr (s == G + 2 && k < 3) { if constexpr (k == 0) gs = __builtin_bit_cast(f32x4, gl[0]); add4(gs, __builtin_bit_cast(f32x4, gl[k + 1])); }
      if constexpr (s == G + 2 && k == 3) *(LAS f32x4*)(lb + ST_WDT + (lane < 60 ? gg * 2560 + gx * 16 : 7680 + (lane - 60) * 16)) = gs;
      if constexpr (s == G + 3 && k == 3) {
        if (w < 3) {
          const unsigned o = (tid < 160 ? tid : 0) * 16;
          f32x4 t = *(LAS f32x4*)(lb + ST_WDT + o);
          add4(t, *(LAS f32x4*)(lb + ST_WDT + 2560 + o)); add4(t, *(LAS f32x4*)(lb + ST_WDT + 5120 + o));
          *(LAS f32x4*)(lb + ST_CSP + o) = t;
        }
      }
      if constexpr (s == 19 && k == 2) { wait_lgkm0(); bar(); }
      if constexpr (s == 19 && k >= 3 && k < 3 + NF) csv[k - 3] = *(LAS f32x4*)(lb + ST_CSP + (p * 6 + k - 3) * 64 + g4 * 16);
      if constexpr (s == 19 && k >= 3 + NF && k < 7 + NF) rsv[k - 3 - NF] = *(LAS f32x2*)(lb + ST_CSP + 1536 + (64 * tw + 16 * (k - 3 - NF) + r) * 8);
      if constexpr (k % 3 == 1 && k / 3 < 5) {
        constexpr int i = k / 3;
        if constexpr (s >= 17) {
          constexpr int n = ROLE ? (s - 17) * 5 + i : 99;
          if constexpr (n < 5) glds_sc1(lb + w * SLOT + n * 1024, l16, b1 + n * 1024);
          else if constexpr (n < 8) {
            constexpr int i0 = n - 5, i1 = i0 == 0 ? 3 : 4;
            glds_sc1(lb + (8 + p) * SLOT + (tw ? i1 : i0) * 1024, l16, b2 + (tw ? i1 : i0) * 1024);
          }
        } else {
          if constexpr (i == 0 && s + 5 <= 19) issue_x(s + 5);
          if constexpr (i >= 1 && i <= 3 && s + NSW <= 19) issue_w(s + NSW, i - 1);
        }
      }
      SB;
    });
  });
  asm volatile("s_nop 15" ::: "memory");
  SB;
  float ve[4], ra[4];
#pragma unroll
  for (int m = 0; m < 4; ++m) {
    const float mean = rsv[m][0] * (1.0f / 640.0f);
    ve[m] = __builtin_fmaxf(rsv[m][1] * (1.0f / 640.0f) - mean * mean, 0.f) + 1e-5f; ra[m] = __builtin_amdgcn_rsqf(ve[m]);
#pragma unroll
    for (int f = 0; f < NF; ++f) acc[m][f] -= csv[f] * mean;
  }

  if constexpr (ROLE) {
    const float k1 = -2.0f * 0.7978845608028654f * 1.4426950408889634f, k3 = k1 * 0.044715f;
    const char* hb = ws + WS_H + (size_t)((8 * mt + 4 * tw) * 48 + 6 * ct) * 1024;
    u32x2 e3[2];
#pragma unroll
    for (int m = 0; m < 4; ++m) {
      const float c1 = ra[m] * k1, c3 = ra[m] * ra[m] * ra[m] * k3;
      u32x2 a = geglu4(acc[m][0], acc[m][1], c1, c3, ve[m]), b = geglu4(acc[m][2], acc[m][3], c1, c3, ve[m]);
      st16(l16, hb + (m * 48 + p) * 1024, (u32x4){a[0], a[1], b[0], b[1]});
      e3[m & 1] = geglu4(acc[m][4], acc[m][5], c1, c3, ve[m]);
      if (m & 1) {
        auto s0 = __builtin_amdgcn_permlane32_swap(e3[0][0], e3[1][0], false, false);
        auto s1 = __builtin_amdgcn_permlane32_swap(e3[0][1], e3[1][1], false, false);
        unsigned a0 = s0[0], b0 = s0[1], a1 = s1[0], b1 = s1[1];
        st16((unsigned)((g4 >> 1) * 49152 + (lane & 31) * 16), hb + ((m - 1) * 48 + 4 + (p >> 1)) * 1024 + (p & 1) * 512, (u32x4){a0, a1, b0, b1});
      }
    }
    wait_vm<0>();
    bar();
    if (tid < 16) __hip_atomic_store((GAS u64*)(ws + WS_FP + (size_t)((mt * 16 + tid) * 16 + 8 + ct) * 8), tag, RLX_AGENT);
  } else {
    const int hd = 4 * ct + p;
    f32x4 x3[4];
#pragma unroll
    for (int m = 0; m < 4; ++m) {
      const unsigned ro = lb + KVI + p * (128 * KVP) + (64 * tw + 16 * m + r) * KVP;
      *(LAS u32x2*)(ro + 8 * g4) = pk4(phi4(acc[m][1] * ra[m] + 1e-6f));
      *(LAS u32x2*)(ro + 40 + 8 * g4) = pk4(acc[m][2] * ra[m]);
      const f32x4 t = acc[m][3] * ra[m];
      x3[m] = phi4(t + 1e-6f);
      const u32x2 e0 = pk4(x3[m]), e1 = pk4(t);
      if (g4 == 1 || g4 == 2) *(LAS u32x2*)(ro + (g4 == 1 ? 32 : 72)) = g4 == 1 ? e0 : e1;
    }
    wait_lgkm0();
    bar();
    {
      f32x4 t0 = {0.f, 0.f, 0.f, 0.f}, t1 = t0;
      const unsigned tb = lb + KVI + p * (128 * KVP) + (4 * g4 + q) * KVP;
      const unsigned kLo = tb + pp * 8, kHi = tb + 32;
      const bool one = tw && pp;
      const unsigned vA = one ? lb + ST_ONE : tb + 40 + (tw ? 32 : pp * 8), vs = one ? 0u : (unsigned)KVP;
#pragma unroll
      for (int ts = 0; ts < 4; ++ts) {
        v8bf kl = cat8(trd(kLo + ts * 32 * KVP), trd(kLo + (ts * 32 + 16) * KVP));
        v8bf kh = cat8(trd(kHi + ts * 32 * KVP), trd(kHi + (ts * 32 + 16) * KVP));
        v8bf vv = cat8(trd(vA + ts * 32 * vs), trd(vA + (ts * 32 + 16) * vs));
        t0 = mfma16(vv, kl, t0); t1 = mfma16(vv, kh, t1);
      }
      asm volatile("s_nop 7\n\ts_nop 7" : "+v"(t0), "+v"(t1));
      const char* kvp = ws + WS_KV + (size_t)(hd * 16 + mt) * 1600;
      const char* zp = ws + WS_ZP + (size_t)(hd * 16 + mt) * 128;
      if (tw == 0 || g4 == 0) {
        const unsigned eo = tw ? 64u : 16u * g4;
        st16(r * 80 + eo, kvp, __builtin_bit_cast(u32x4, t0));
        if (r < 4) st16((16 + r) * 80 + eo, kvp, __builtin_bit_cast(u32x4, t1));
      } else if (g4 == 1) {
        st4(r * 4, zp, t0[0]);
        if (r < 4) st4(64 + r * 4, zp, t1[0]);
      }
    }
    if (tw) wait_vm<4>(); else wait_vm<2>();
    bar();
    if (w >= 4) {
#pragma unroll
      for (int i = 0; i < 35; ++i) glds_sc1(lb + (4 * (i / 5) + p) * SLOT + (i % 5) * 1024, l16, b2 + (i - 10) * 1024);
    }
    if (w < 2) __builtin_amdgcn_s_setprio(3);
    f32x4 pq[4];
#pragma unroll
    for (int m = 0; m < 4; ++m) { pq[m] = phi4(acc[m][0] * ra[m] + 1e-6f); asm volatile("" : "+v"(pq[m])); }
    const int h = 4 * ct + (mt & 3);
    f32x4 z0, z1;
    if (tw == 0) {
      const char* zsrc = ws + WS_ZP + (size_t)hd * 2048;
      __builtin_amdgcn_s_sleep(ZFIRST);
      for (unsigned spins = 0; spins < (1u << 18); ++spins) {
        u32x4 a, b;
        gld16_sc1<0>(a, r * 128 + g4 * 16, zsrc); gld16_sc1<64>(b, r * 128, zsrc);
        asm volatile("s_waitcnt vmcnt(0)" : "+v"(a), "+v"(b) :: "memory");
        z0 = __builtin_bit_cast(f32x4, a); z1 = __builtin_bit_cast(f32x4, b);
        const f32x4 t4 = z0 + z1; const float t1 = (t4[0] + t4[1]) + (t4[2] + t4[3]);
        if (!__any(t1 != t1)) break;
        __builtin_amdgcn_s_sleep(ZSLEEP);
      }
#pragma unroll
      for (int j = 0; j < 4; ++j) { z0[j] = rsum16(z0[j]); z1[j] = rsum16(z1[j]); }
      if (r == 0) { *(LAS f32x4*)(lb + P2_FLG + 128 + p * 128 + g4 * 16) = z0; *(LAS f32x4*)(lb + P2_FLG + 128 + p * 128 + 64) = z1; }
      wait_lgkm0();
      if (lane == 0) __hip_atomic_fetch_or((LAS unsigned*)(lb + P2_FLG + 24), 1u << p, __ATOMIC_RELAXED, __HIP_MEMORY_SCOPE_WORKGROUP);
    } else {
      wait_lds(lb + P2_FLG + 24, 1u << p);
      z0 = *(LAS f32x4*)(lb + P2_FLG + 128 + p * 128 + g4 * 16); z1 = *(LAS f32x4*)(lb + P2_FLG + 128 + p * 128 + 64);
    }
    u32x4 ka[16], kb[16];
    const char* kp = ws + WS_KV + (size_t)h * 16 * 1600;
    const unsigned l2 = (lane < 36 ? 64 + lane : 64) * 16;
    if (w < 2) sfor<16>([&](auto I) { gld16_sc1<I * 1600>(ka[I], l16, kp); gld16_sc1<I * 1600>(kb[I], l2, kp); });
    if (g4 != 0) z1 = (f32x4){0.f, 0.f, 0.f, 0.f};
    {
      u32x2 e[4], e3[4];
#pragma unroll
      for (int m = 0; m < 4; ++m) {
        const f32x4 t = pq[m] * z0 + x3[m] * z1;
        const float rd = __builtin_amdgcn_rcpf(xsum4((t[0] + t[1]) + (t[2] + t[3])) + 1e-6f);
        e[m] = pk4(pq[m] * rd); e3[m] = pk4(x3[m] * rd);
      }
      const int hl = hd & 7;
      const char* qb = ws + WS_PQ + (size_t)((8 * mt + 4 * tw) * 20 + 5 * (hd >> 3)) * 1024;
#pragma unroll
      for (int m = 0; m < 4; m += 2) {
        auto s0 = __builtin_amdgcn_permlane32_swap(e[m][0], e[m + 1][0], false, false);
        auto s1 = __builtin_amdgcn_permlane32_swap(e[m][1], e[m + 1][1], false, false);
        unsigned a0 = s0[0], b0 = s0[1], a1 = s1[0], b1 = s1[1];
        st16((unsigned)((m + (g4 >> 1)) * 20 * 1024 + (r + 16 * (2 * (hl & 1) + (g4 & 1))) * 16), qb + (hl >> 1) * 1024, (u32x4){a0, a1, b0, b1});
      }
      if (g4 == 0) {
#pragma unroll
        for (int m = 0; m < 4; ++m) st8((unsigned)(hl * 128 + r * 8), qb + (m * 20 + 4) * 1024, e3[m]);
      }
    }
    if (w < 2) {
      const int hl = h & 7;
      const unsigned my = lb + PRS + w * 8192, wl = lb + PRS + 1024 + w * 160;
      f32x4 sa, sb;
      for (unsigned spins = 0; spins < (1u << 18); ++spins) {
        wait_vm4<0>(ka[0], ka[1], ka[2], ka[3]);
        pin4(ka[4], ka[5], ka[6], ka[7]); pin4(ka[8], ka[9], ka[10], ka[11]); pin4(ka[12], ka[13], ka[14], ka[15]);
        pin4(kb[0], kb[1], kb[2], kb[3]); pin4(kb[4], kb[5], kb[6], kb[7]); pin4(kb[8], kb[9], kb[10], kb[11]); pin4(kb[12], kb[13], kb[14], kb[15]);
        sa = __builtin_bit_cast(f32x4, ka[0]); sb = __builtin_bit_cast(f32x4, kb[0]);
#pragma unroll
        for (int i = 1; i < 16; ++i) { sa += __builtin_bit_cast(f32x4, ka[i]); sb += __builtin_bit_cast(f32x4, kb[i]); }
        const f32x4 t4 = sa + sb; const float t1 = (t4[0] + t4[1]) + (t4[2] + t4[3]);
        if (__builtin_expect(!__any(t1 != t1), 1)) break;
        sfor<16>([&](auto I) { gld16_sc1r<I * 1600>(ka[I], l16, kp); gld16_sc1r<I * 1600>(kb[I], l2, kp); });
        wait_vm<0>();
      }
      *(LAS u32x2*)(my + lane * 8) = pk4(sa);
      *(LAS u32x2*)(my + 512 + lane * 8) = pk4(sb);
      u32x4 Ad[2];
#pragma unroll
      for (int mf = 0; mf < 2; ++mf) {
        int d = mf == 0 ? r : 16 + (r & 3);
        u32x2 lo = *(LAS u32x2*)(my + d * 40 + g4 * 8);
        u32x2 hi = *(LAS u32x2*)(my + d * 40 + 32);
        Ad[mf] = (u32x4){lo[0], lo[1], g4 == 0 ? hi[0] : 0u, g4 == 0 ? hi[1] : 0u};
      }
      const char* mb = ws + WS_M + (size_t)((2 * (mt >> 2) + w) * 100 + 25 * (h >> 3)) * 1024;
      const unsigned o0 = (hl >> 1) * 5120 + (r + 16 * (2 * (hl & 1) + (g4 & 1))) * 16 + (g4 >> 1) * 8;
      const unsigned o1 = 4 * 5120 + (r + 16 * (hl & 3)) * 16 + (hl >> 2) * 8;
#pragma unroll
      for (int f = 0; f < 5; ++f) {
        v8bf B = cat8(trd(wl + (4 * g4 + q) * 320 + pp * 8 + f * 32), trd(wl + (16 + q) * 320 + pp * 8 + f * 32));
        f32x4 z4 = {0.f, 0.f, 0.f, 0.f};
        f32x4 d0 = mfma16(__builtin_bit_cast(v8bf, Ad[0]), B, z4), d1 = mfma16(__builtin_bit_cast(v8bf, Ad[1]), B, z4);
        const char* dst = mb + f * 1024;
        st8(o0, dst, pk4(d0));
        if (g4 == 0) st8(o1, dst, pk4(d1));
      }
      __builtin_amdgcn_s_setprio(0);
    } else wait_vm<0>();
    {
      unsigned old = 0;
      if (lane == 0) old = __hip_atomic_fetch_add((LAS unsigned*)(lb + P2_FLG + 20), 1u, __ATOMIC_RELAXED, __HIP_MEMORY_SCOPE_WORKGROUP);
      if (__builtin_amdgcn_readfirstlane(old) == 7 && lane < 16)
        __hip_atomic_store((GAS u64*)(ws + WS_FP + (size_t)((mt * 16 + lane) * 16 + ct) * 8), tag, RLX_AGENT);
    }
  }
}

__global__ __launch_bounds__(512, 2) void fused_kernel(const bf16* __restrict__ x, const bf16* __restrict__ wqkv,
                                                       const bf16* __restrict__ wo, const bf16* __restrict__ wg,
                                                       const bf16* __restrict__ wu, const bf16* __restrict__ wdn,
                                                       char* __restrict__ ws, bf16* __restrict__ out) {
  __shared__ __attribute__((aligned(16))) char smem[LDS_BYTES];
  Ctx C;
  C.x = x; C.wqkv = wqkv; C.wo = wo; C.wg = wg; C.wu = wu; C.wdn = wdn; C.ws = ws;
  const unsigned lb = (unsigned)(uintptr_t)(LAS char*)smem;
  const int tid = threadIdx.x, lane = tid & 63;
  const int w = __builtin_amdgcn_readfirstlane(tid >> 6);
  const int r = lane & 15, g4 = lane >> 4;
  const int bid = blockIdx.x, xcd = bid & 7, idx = bid >> 3;
  const unsigned l16 = lane * 16;
  const int role = (idx >> 4) ^ 1;
  const int mt2 = 4 * xcd + (idx >> 3), c = idx & 7;
  C.lb = lb; C.l16 = l16; C.tid = tid; C.lane = lane; C.w = w; C.tw = w >> 2; C.p = w & 3; C.r = r; C.g4 = g4;
  C.q = (lane & 15) >> 2; C.pp = lane & 3; C.bid = bid; C.xcd = xcd; C.idx = idx;
  C.mt = 8 * (xcd >> 2) + (idx & 7); C.ct = 2 * (xcd & 3) + ((idx >> 3) & 1); C.mt2 = mt2; C.c = c;
  {
    u64 pc; unsigned len;
    asm volatile("s_getpc_b64 %0\n.Lfk_begin:\n\ts_mov_b32 %1, .Lfk_end-.Lfk_begin" : "=s"(pc), "=s"(len));
    unsigned off = (unsigned)((idx * 8 + w) * 3 + (lane < 3 ? lane : 0)) * 128; off = off < len ? off : 0;
    asm volatile("s_mov_b32 m0, %0\n\ts_nop 0\n\tglobal_load_lds_dword %1, %2" ::"s"(lb + ST_SINK + w * 256), "v"(off), "s"(pc) : "memory");
  }
  if (tid < 8) *(LAS unsigned*)(lb + P2_FLG + tid * 4) = 0u;
  if (tid < 16) *(LAS unsigned*)(lb + ST_ONE + tid * 4) = 0x3f803f80u;

  u64 tag;
  if (role) phase1<1>(C, tag); else phase1<0>(C, tag);

  f32x4 acc[4][5];
  auto zero_acc = [&]() {
#pragma unroll
    for (int m = 0; m < 4; ++m) {
#pragma unroll
      for (int f = 0; f < 5; ++f) acc[m][f] = (f32x4){0.f, 0.f, 0.f, 0.f};
      asm volatile("" : "+v"(acc[m][0]), "+v"(acc[m][1]), "+v"(acc[m][2]), "+v"(acc[m][3]), "+v"(acc[m][4]));
    }
    asm volatile("s_nop 1" ::: "memory");
  };
  u32x2 xres[3];
  auto load_x = [&]() {
#pragma unroll
    for (int i = 0; i < 3; ++i) {
      int t = w + 8 * i; t = t < 20 ? t : w;
      const int m = t / 5, f = t - 5 * m;
      xres[i] = *(const u32x2*)(x + (size_t)(64 * mt2 + 16 * m + r) * 640 + 80 * c + 16 * f + 4 * g4);
    }
    asm volatile("" ::: "memory");
  };

  if (w >= 4) {
    const int j = w - 4;
    const char* bp = ws + WS_WD + (size_t)((c * 48 + 12 * j) * 5) * 1024;
    const char* ap = ws + WS_H + ((size_t)(4 * mt2) * 48 + 12 * j) * 1024;
    u32x4 A[3][4], Br[3][5], Bc[2][5];
    auto issue_a = [&](auto SC) {
      constexpr int s = SC;
      sfor<4>([&](auto M) { gld16_sc1<(M * 48 + s) * 1024>(A[s % 3][M], l16, ap); });
    };
    unsigned rb[3];
    rb[0] = lb + j * SLOT + l16; rb[1] = rb[0] + 12 * SLOT; rb[2] = rb[0] + 24 * SLOT;
    asm volatile("" : "+v"(rb[1]), "+v"(rb[2]));
    auto rdB = [&](u32x4 (&B)[5], int pos) {
#pragma unroll
      for (int f = 0; f < 5; ++f) B[f] = *(LAS u32x4*)(rb[pos / 3] + 4 * (pos % 3) * SLOT + f * 1024);
    };
    sfor<15>([&](auto I) { gld16_sc1<(35 + I) * 1024>(Br[I / 5][I % 5], l16, bp); });
    wait_lds(lb + P2_FLG + 16, 0x300u << (2 * j));
    sfor<3>([&](auto SC) { issue_a(SC); });
    zero_acc();
    wait_vm4<12>(Br[0][0], Br[0][1], Br[0][2], Br[0][3]);
    pin4(Br[0][4], Br[1][0], Br[1][1], Br[1][2]); pin4(Br[1][3], Br[1][4], Br[2][0], Br[2][1]); pin1(Br[2][2]); pin1(Br[2][3]); pin1(Br[2][4]);
    rdB(Bc[0], 0);
    sfor<12>([&](auto SC) {
      constexpr int s = SC; constexpr int b = s % 3;
      constexpr auto gs = [](int g) { return g <= 11 ? (g == 9 ? 14 : 4) : 0; };
      SB;
      wait_vm4<gs(s + 1) + gs(s + 2)>(A[b][0], A[b][1], A[b][2], A[b][3]);
      if constexpr (s >= 10) { pin4(Bc[s & 1][0], Bc[s & 1][1], Bc[s & 1][2], Bc[s & 1][3]); pin1(Bc[s & 1][4]); }
      if constexpr (s + 1 <= 6) rdB(Bc[(s + 1) & 1], s + 1);
      SB;
#pragma unroll
      for (int f = 0; f < 5; ++f) {
#pragma unroll
        for (int m = 0; m < 4; ++m) {
          mfma_au(acc[m][f], (s >= 7 && s <= 9) ? Br[(s >= 7 && s <= 9) ? s - 7 : 0][f] : Bc[s & 1][f], A[b][m]);
          if (s == 11 && f >= 2) {
            SB; *(LAS f32x4*)(lb + (4 * m + j) * SLOT + (f - 2) * 1024 + l16) = acc[m][f - 2]; SB;
          }
        }
      }
      SB;
      if constexpr (s == 6) {
        if (lane == 0) *(volatile LAS unsigned*)(lb + P2_FLG + j * 4) = 1u;
        sfor<10>([&](auto I) { gld16_sc1<(50 + I) * 1024>(Bc[I / 5][I % 5], l16, bp); });
      }
      if constexpr (s + 3 < 12) issue_a(IC(s + 3));
      if constexpr (s == 9) load_x();
    });
    asm volatile("s_nop 15" ::: "memory");
    SB;
#pragma unroll
    for (int m = 0; m < 4; ++m)
#pragma unroll
      for (int f = 3; f < 5; ++f) *(LAS f32x4*)(lb + (4 * m + j) * SLOT + f * 1024 + l16) = acc[m][f];
  } else {
    const int wq = w;
    if (wq == 3) {
      const char* fpan = ws + WS_FP + (size_t)(((mt2 >> 1) * 16 + (mt2 & 1) * 8 + c) * 16) * 8;
      for (unsigned spins = 0; spins < (1u << 20); ++spins) {
        u64 v1 = __hip_atomic_load((GAS u64*)(fpan + r * 8), RLX_AGENT);
        const unsigned st = (unsigned)__ballot(v1 == tag) & 0xffffu;
        asm volatile("" ::: "memory");
        if (lane == 0) *(volatile LAS unsigned*)(lb + P2_FLG + 16) = st;
        if (st == 0xffffu) break;
        __builtin_amdgcn_s_sleep(PSLEEP);
      }
    }
    load_x();
    wait_lds(lb + P2_FLG + 16, 3u << (2 * wq));
    const char* pq_ = ws + WS_PQ + ((size_t)(4 * mt2) * 20 + 5 * wq) * 1024;
    const char* mp = ws + WS_M + ((size_t)c * 100 + 25 * wq) * 1024;
    for (unsigned spins = 0; spins < (1u << 16); ++spins) {
      u32x4 P[3][4], Mf[3][5];
      auto issue = [&](auto SC) {
        constexpr int s = SC;
        sfor<5>([&](auto F) { gld16_sc1<(5 * s + F) * 1024>(Mf[s % 3][F], l16, mp); });
        if constexpr (s < 4) sfor<4>([&](auto M) { gld16_sc1<(M * 20 + s) * 1024>(P[s % 3][M], l16, pq_); });
        else sfor<4>([&](auto M) { gld8x2_sc1(P[s % 3][M], lane * 8, pq_ + (M * 20 + 4) * 1024); });
      };
      sfor<3>([&](auto SC) { issue(SC); });
      zero_acc();
      sfor<5>([&](auto SC) {
        constexpr int s = SC, b = s % 3;
        SB;
        wait_vm4<(s <= 1 ? 18 : (s == 2 ? 22 : (s == 3 ? 13 : 0)))>(Mf[b][0], Mf[b][1], Mf[b][2], Mf[b][3]); pin1(Mf[b][4]);
        pin4(P[b][0], P[b][1], P[b][2], P[b][3]);
#pragma unroll
        for (int f = 0; f < 5; ++f)
#pragma unroll
          for (int m = 0; m < 4; ++m) mfma_au(acc[m][f], Mf[b][f], P[b][m]);
        SB;
        if constexpr (s < 2) issue(IC(s + 3));
      });
      asm volatile("s_nop 15" ::: "memory");
      SB;
      const f32x4 t4 = (acc[0][0] + acc[0][1]) + (acc[0][2] + acc[0][3]) + acc[0][4];
      const float t1 = (t4[0] + t4[1]) + (t4[2] + t4[3]);
      if (__builtin_expect(!__any(t1 != t1), 1)) break;
      __builtin_amdgcn_s_sleep(8);
    }
    if (wq != 0) {
      for (unsigned spins = 0; spins < (1u << 24); ++spins) {
        unsigned v = *(volatile LAS unsigned*)(lb + P2_FLG + (lane & 3) * 4);
        if (__all(v == 1u)) break;
        __builtin_amdgcn_s_sleep(1);
      }
      asm volatile("" ::: "memory");
    }
    if (wq == 0 && !role) wait_lds(lb + P2_FLG + 20, 8u);
    const unsigned pa = wq == 0 ? lb + P2_SCR : lb + (16 + 4 * (wq - 1)) * SLOT;
#pragma unroll
    for (int m = 0; m < 4; ++m)
#pragma unroll
      for (int f = 0; f < 5; ++f)
        if (m * 5 + f != 16 || wq != 0) *(LAS f32x4*)(pa + (m * 5 + f) * 1024 + l16) = acc[m][f];
  }
  wait_lgkm0();
  bar();
  f32x4 ps[3][4], pd[3][4];
#pragma unroll
  for (int i = 0; i < 3; ++i) {
    const int t = (i < 2 || w < 4) ? w + 8 * i : w, m = t / 5, f = t - 5 * m;
    ps[i][0] = *(LAS f32x4*)(lb + P2_SCR + t * 1024 + l16);
#pragma unroll
    for (int k = 0; k < 4; ++k) {
      if (k) ps[i][k] = *(LAS f32x4*)(lb + (16 + 4 * (k - 1)) * SLOT + t * 1024 + l16);
      pd[i][k] = *(LAS f32x4*)(lb + (4 * m + k) * SLOT + f * 1024 + l16);
    }
  }
  if (w == 0) ps[2][0] = acc[3][1];
#pragma unroll
  for (int i = 0; i < 3; ++i) {
    const int t = w + 8 * i;
    if (i < 2 || w < 4) {
      const int m = t / 5, f = t - 5 * m;
      const f32x4 s = (ps[i][0] + ps[i][1]) + (ps[i][2] + ps[i][3]), d = (pd[i][0] + pd[i][1]) + (pd[i][2] + pd[i][3]);
      float xo[4] = {blo(xres[i][0]), bhi(xres[i][0]), blo(xres[i][1]), bhi(xres[i][1])};
      float res[4];
#pragma unroll
      for (int jj = 0; jj < 4; ++jj) {
        float g = d[jj] * __builtin_amdgcn_rcpf(1.0f + __expf(-d[jj]));
        res[jj] = __builtin_fmaf(s[jj], g, xo[jj]);
      }
      *(u32x2*)(out + (size_t)(64 * mt2 + 16 * m + r) * 640 + 80 * c + 16 * f + 4 * g4) = (u32x2){pk2(res[0], res[1]), pk2(res[2], res[3])};
    }
  }
  asm volatile(".Lfk_end:" ::: "memory");
}

extern "C" void kernel_launch(void* const* d_in, const int* in_sizes, int n_in, void* d_out, int out_size, void* d_ws,
                              size_t ws_size, hipStream_t stream) {
  fused_kernel<<<256, 512, 0, stream>>>((const bf16*)d_in[0], (const bf16*)d_in[1], (const bf16*)d_in[2], (const bf16*)d_in[4],
                                        (const bf16*)d_in[5], (const bf16*)d_in[6], (char*)d_ws, (bf16*)d_out);
}
```
